```python
import torch

def _softmax(x, axis=-1):
    return torch.softmax(x, dim=axis)
def _gelu(x):
    return torch.nn.functional.gelu(x, approximate="tanh")
def _silu(x):
    return torch.nn.functional.silu(x)
def _sigmoid(x):
    return torch.sigmoid(x)
def _layernorm(x, axis=-1, eps=1e-5):
    m = x.mean(dim=axis, keepdim=True)
    v = x.var(dim=axis, keepdim=True, unbiased=False)
    return (x - m) / torch.sqrt(v + eps)
def _scatter_add(tgt, idx, src, axis=0):
    return tgt.clone().index_add_(axis, idx, src)
def _segment_sum(src, seg, n):
    out = torch.zeros((n,) + src.shape[1:], dtype=src.dtype)
    return out.index_add_(0, seg, src)
def _pad(x, pw):
    flat = tuple(v for pair in reversed(pw) for v in pair)
    return torch.nn.functional.pad(x, flat)

kernel_name = 'gated_mix__linear_kernel_hedgehog__ssm_conv_s4__prompt'

def setup_inputs():

    _w = (lambda a: (lambda t: t.to(torch.bfloat16) if t.is_floating_point() else t)(torch.as_tensor(a)))
    def _A_setup():
        B, S, D, H = 1, 2048, 512, 32
        Dh = D // H
        return {
            "x": _w((lambda *s: torch.randn(*s, dtype=torch.bfloat16) * 0.05)(B, S, D)),
            "wqkv": _w((lambda *s: torch.randn(*s, dtype=torch.bfloat16) * 0.8)(D, 3 * D)),
            "w_phi": _w((lambda *s: torch.randn(*s, dtype=torch.bfloat16) * 0.8)(H, Dh, Dh) * (Dh ** -0.5)),
            "wo": _w((lambda *s: torch.randn(*s, dtype=torch.bfloat16) * 0.05)(D, D)),
        }
    _A_in = _A_setup()

    def _B_setup():
        B, L, D, N = 1, 2048, 512, 128
        A_re = -(lambda _x: torch.exp(torch.as_tensor(_x)))((lambda *s: torch.randn(*s, dtype=torch.bfloat16) * 3.2)(D, N))
        A_im = torch.pi * (lambda *s: torch.randn(*s, dtype=torch.bfloat16) * 0.05)(D, N)
        B_re = (lambda *s: torch.randn(*s, dtype=torch.bfloat16) * 0.2)(D, N)
        B_im = (lambda *s: torch.randn(*s, dtype=torch.bfloat16) * 0.2)(D, N)
        C_re = (lambda *s: torch.randn(*s, dtype=torch.bfloat16) * 0.2)(D, N)
        C_im = (lambda *s: torch.randn(*s, dtype=torch.bfloat16) * 0.2)(D, N)
        log_dt = (lambda *s: torch.randn(*s, dtype=torch.bfloat16) * 0.05)(D) * 0.5 - 4.0
        Dskip = (lambda *s: torch.randn(*s, dtype=torch.bfloat16) * 0.05)(D)
        return {
            "u": _w((lambda *s: torch.randn(*s, dtype=torch.bfloat16) * 0.05)(B, L, D)),
            "A_re": _w(A_re),
            "A_im": _w(A_im),
            "B_re": _w(B_re),
            "B_im": _w(B_im),
            "C_re": _w(C_re),
            "C_im": _w(C_im),
            "log_dt": _w(log_dt),
            "Dskip": _w(Dskip),
        }
    _B_in = _B_setup()

    return {
        "A_x": _A_in["x"],
        "A_wqkv": _A_in["wqkv"],
        "A_w_phi": _A_in["w_phi"],
        "A_wo": _A_in["wo"],
        "B_u": _B_in["u"],
        "B_A_re": _B_in["A_re"],
        "B_A_im": _B_in["A_im"],
        "B_B_re": _B_in["B_re"],
        "B_B_im": _B_in["B_im"],
        "B_C_re": _B_in["C_re"],
        "B_C_im": _B_in["C_im"],
        "B_log_dt": _B_in["log_dt"],
        "B_Dskip": _B_in["Dskip"],
        "gate_w": _w((lambda *s: torch.randn(*s, dtype=torch.bfloat16) * 0.05)(512) * 16.0),
    }

def reference(A_x, A_wqkv, A_w_phi, A_wo, B_u, B_A_re, B_A_im, B_B_re, B_B_im, B_C_re, B_C_im, B_log_dt, B_Dskip, gate_w):
    def _A_ref(x, wqkv, w_phi, wo):
        B, S, D = x.shape
        H = 32; Dh = D // H
        qkv = (lambda x, *d: x.permute(*d))((x @ wqkv).reshape(B, S, 3, H, Dh), 2, 0, 3, 1, 4)
        q, k, v = qkv[0], qkv[1], qkv[2]
        pq = torch.einsum("bhsd,hdf->bhsf", q, w_phi)
        pk = torch.einsum("bhsd,hdf->bhsf", k, w_phi)
        phi_q = torch.concatenate([_softmax(pq, axis=-1), _softmax(-pq, axis=-1)], axis=-1)
        phi_k = torch.concatenate([_softmax(pk, axis=-1), _softmax(-pk, axis=-1)], axis=-1)
        kv = torch.einsum("bhsf,bhsd->bhfd", phi_k, v)
        z  = torch.einsum("bhsf->bhf", phi_k)
        num = torch.einsum("bhqf,bhfd->bhqd", phi_q, kv)
        den = torch.einsum("bhqf,bhf->bhq", phi_q, z)[..., None] + 1e-6
        o = num / den
        return (lambda x, *d: x.permute(*d))(o, 0, 2, 1, 3).reshape(B, S, D) @ wo

    def _B_ref(u, A_re, A_im, B_re, B_im, C_re, C_im, log_dt, Dskip):
        Bsz, L, D = u.shape
        dt = torch.exp(log_dt)[:, None]
        A = A_re + 1j * A_im
        Bc = B_re + 1j * B_im
        Cc = C_re + 1j * C_im
        logdA = dt * A
        dB = dt * Bc
        l = (lambda x, dt: x.to(dt))(torch.arange(L), A_re.dtype)
        P = torch.exp(logdA[:, :, None] * l[None, None, :])
        K = torch.einsum("dn,dnl,dn->dl", Cc, P, dB).real
        L2 = 2 * L
        Kp = _pad(K, ((0, 0), (0, L)))
        up = (lambda x, *d: x.permute(*d))(u, 0, 2, 1)
        up = _pad(up, ((0, 0), (0, 0), (0, L)))
        Kf = (lambda x, axes=(-2,-1): torch.fft.rfft2(x.float(), dim=axes))(Kp[:, None, :], axes=(-2, -1))
        Uf = (lambda x, axes=(-2,-1): torch.fft.rfft2(x.float(), dim=axes))(up[:, :, None, :], axes=(-2, -1))
        Yf = Uf * Kf[None, :, :, :]
        y = (lambda x, s, axes=(-2,-1): torch.fft.irfft2(x, s=s, dim=axes).to(torch.bfloat16))(Yf, (1, L2), axes=(-2, -1))[:, :, 0, :L]
        y = (lambda x, *d: x.permute(*d))(y, 0, 2, 1) + u * Dskip[None, None, :]
        return y

    x = A_x
    g = _sigmoid((x * gate_w).sum(-1, keepdims=True))
    a = _A_ref(x, A_wqkv, A_w_phi, A_wo)
    b = _B_ref(x, B_A_re, B_A_im, B_B_re, B_B_im, B_C_re, B_C_im, B_log_dt, B_Dskip)
    return g * a + (1 - g) * b
```

```cpp
#include <hip/hip_runtime.h>
#include <hip/hip_bf16.h>
typedef __hip_bfloat16 bf16;
#define S_ 2048
#define D_ 512
#define H_ 32
#define N_ 128
__device__ __forceinline__ float b2f(bf16 x){ return __bfloat162float(x);}
__device__ __forceinline__ float rbf(float x){ return __bfloat162float(__float2bfloat16(x));}

__global__ void k_qkv(const bf16* x, const bf16* w, float* qkv){
  int t = blockIdx.x;
  for(int c = threadIdx.x; c < 1536; c += blockDim.x){
    float acc=0; for(int k=0;k<D_;k++) acc += b2f(x[t*D_+k])*b2f(w[k*1536+c]);
    qkv[t*1536+c]=rbf(acc);
  }
}
__global__ void k_phi(const float* qkv, const bf16* wphi, float* phi){
  int t = blockIdx.x; int tid = threadIdx.x;
  int which = tid>>5, h = tid&31;
  float p[16];
  #pragma unroll
  for(int f=0;f<16;f++){ float a=0;
    #pragma unroll
    for(int d=0;d<16;d++) a += qkv[t*1536+which*512+h*16+d]*b2f(wphi[h*256+d*16+f]); p[f]=rbf(a);}
  float mx=-1e30f, mn=1e30f;
  #pragma unroll
  for(int f=0;f<16;f++){mx=fmaxf(mx,p[f]); mn=fminf(mn,p[f]);}
  float s1=0,s2=0;
  #pragma unroll
  for(int f=0;f<16;f++){s1+=expf(p[f]-mx); s2+=expf(mn-p[f]);}
  #pragma unroll
  for(int f=0;f<16;f++){ phi[((which*S_+t)*H_+h)*32+f]=rbf(expf(p[f]-mx)/s1); phi[((which*S_+t)*H_+h)*32+16+f]=rbf(expf(mn-p[f])/s2);}
}
__global__ void k_kv(const float* qkv, const float* phi, float* kv){
  int h = blockIdx.x; int tid=threadIdx.x;
  if(tid>=544) return;
  int f = tid/17, d = tid%17;
  float a=0;
  for(int t=0;t<S_;t++){ float pk = phi[((1*S_+t)*H_+h)*32+f]; float v = d<16? qkv[t*1536+1024+h*16+d] : 1.f; a+=pk*v;}
  kv[h*544+tid]=rbf(a);
}
__global__ void k_o(const float* phi, const float* kv, float* o){
  int t = blockIdx.x; int c = threadIdx.x; int h=c>>4, d=c&15;
  float num=0, den=0;
  for(int f=0;f<32;f++){ float pq = phi[((0*S_+t)*H_+h)*32+f]; num+=pq*kv[h*544+f*17+d]; den+=pq*kv[h*544+f*17+16];}
  num=rbf(num); den=rbf(rbf(den)+1e-6f);
  o[t*D_+c]=rbf(num/den);
}
__global__ void k_K(const bf16* Are,const bf16* Aim,const bf16* Bre,const bf16* Bim,const bf16* Cre,const bf16* Cim,const bf16* logdt, float* K){
  int d = blockIdx.x;
  float dt = rbf(expf(b2f(logdt[d])));
  for(int l=threadIdx.x;l<S_;l+=blockDim.x){
    float lf = rbf((float)l);
    float acc=0;
    for(int n=0;n<N_;n++){
      float ar=dt*b2f(Are[d*N_+n]), ai=dt*b2f(Aim[d*N_+n]);
      float br=dt*b2f(Bre[d*N_+n]), bi=dt*b2f(Bim[d*N_+n]);
      float cr=b2f(Cre[d*N_+n]), ci=b2f(Cim[d*N_+n]);
      float wr=cr*br-ci*bi, wi=cr*bi+ci*br;
      float e=expf(ar*lf); float s,c; sincosf(ai*lf,&s,&c);
      acc += e*(wr*c - wi*s);
    }
    K[d*S_+l]=acc;
  }
}
__global__ void k_out(const bf16* x,const float* o,const bf16* wo,const float* K,const bf16* Dskip,const bf16* gw, bf16* out){
  int t=blockIdx.x; int c=threadIdx.x;
  __shared__ float red[512];
  red[c]=rbf(b2f(x[t*D_+c])*b2f(gw[c]));
  __syncthreads();
  for(int s=256;s>0;s>>=1){ if(c<s) red[c]+=red[c+s]; __syncthreads();}
  float gs=rbf(red[0]); float g=rbf(1.f/(1.f+expf(-gs)));
  float a=0; for(int k=0;k<D_;k++) a+=o[t*D_+k]*b2f(wo[k*D_+c]); a=rbf(a);
  float y=0; for(int l=0;l<=t;l++) y+=K[c*S_+l]*b2f(x[(t-l)*D_+c]); y=rbf(y);
  float b=rbf(y+rbf(b2f(x[t*D_+c])*b2f(Dskip[c])));
  float r = rbf(g*a)+rbf(rbf(1.f-g)*b);
  out[t*D_+c]=__float2bfloat16(r);
}
extern "C" void kernel_launch(void* const* d_in, const int* in_sizes, int n_in,
                              void* d_out, int out_size, void* d_ws, size_t ws_size,
                              hipStream_t stream) {
  const bf16* x=(const bf16*)d_in[0]; const bf16* wqkv=(const bf16*)d_in[1]; const bf16* wphi=(const bf16*)d_in[2]; const bf16* wo=(const bf16*)d_in[3];
  float* ws=(float*)d_ws;
  float* qkv=ws; float* phi=qkv+S_*1536; float* kv=phi+2*S_*H_*32; float* o=kv+32*544; float* K=o+S_*D_;
  k_qkv<<<S_,256,0,stream>>>(x,wqkv,qkv);
  k_phi<<<S_,64,0,stream>>>(qkv,wphi,phi);
  k_kv<<<32,576,0,stream>>>(qkv,phi,kv);
  k_o<<<S_,512,0,stream>>>(phi,kv,o);
  k_K<<<512,256,0,stream>>>((const bf16*)d_in[5],(const bf16*)d_in[6],(const bf16*)d_in[7],(const bf16*)d_in[8],(const bf16*)d_in[9],(const bf16*)d_in[10],(const bf16*)d_in[11],K);
  k_out<<<S_,512,0,stream>>>(x,o,wo,K,(const bf16*)d_in[12],(const bf16*)d_in[13],(bf16*)d_out);
}
```

```cpp
#include <hip/hip_runtime.h>
#include <hip/hip_bf16.h>
#include <stdint.h>
#pragma clang diagnostic ignored "-Wint-to-pointer-cast"

#define LAS __attribute__((address_space(3)))
#define GAS __attribute__((address_space(1)))
#define DI __device__ __forceinline__
typedef __bf16 bf16x8 __attribute__((ext_vector_type(8)));
typedef __bf16 bf16x4 __attribute__((ext_vector_type(4)));
typedef __bf16 bf16x2 __attribute__((ext_vector_type(2)));
typedef float f32x16 __attribute__((ext_vector_type(16)));
typedef float f32x4 __attribute__((ext_vector_type(4)));
typedef float f32x2 __attribute__((ext_vector_type(2)));
typedef unsigned u32x4 __attribute__((ext_vector_type(4)));
typedef unsigned u32x2 __attribute__((ext_vector_type(2)));
typedef unsigned short u16;

#define FUSED 1
#define WARM 1

constexpr unsigned WS_E = 0;
constexpr unsigned FS = 128;
constexpr unsigned WS_FX = 4096;
constexpr unsigned WS_FY = WS_FX + 256 * FS;
constexpr unsigned WS_FO = WS_FY + 256 * FS;
constexpr unsigned WS_L2 = WS_FO + 256 * FS;
constexpr unsigned WS_L1Z = WS_L2 + 8 * 32 * 272;
constexpr unsigned WS_L1K = WS_L1Z + 8 * 32 * 512;
constexpr unsigned WS_XT = WS_L1K + 8 * 32 * 32 * 256;
constexpr unsigned WS_Y = WS_XT + 512 * 4096 + 4096;
constexpr unsigned WS_O = WS_Y + 32 * 512 * 128;
constexpr unsigned WS_ST = WS_O + 128 * 16 * 1024;
constexpr unsigned WS_END = WS_ST + 512 * 4 * 16 * 8;

constexpr unsigned SLOT = 24576;
constexpr unsigned A_XCH = SLOT + 16384;
constexpr unsigned A_WPHI = 3 * SLOT;
constexpr unsigned A_GW = A_WPHI + 2048;
constexpr unsigned A_XZ = A_GW + 1024;
constexpr unsigned A_GP = A_XZ + 512;
constexpr unsigned A_DUMMY = A_GP + 1024;
constexpr unsigned A_WO = 0, A_Y = 65536;
constexpr unsigned TROW = 528, TSZ = 32 * TROW;
constexpr unsigned S_KR = 4 * TSZ, KSZ = 4608;
constexpr unsigned S_XT = S_KR;
constexpr unsigned UCP = 4160, USZ = 4 * UCP;
constexpr unsigned S_RED = 2 * USZ;
constexpr unsigned LDS_BYTES = 79872;
constexpr int SPIN_CAP = 1 << 17;

struct Params {
  const u16 *x, *wqkv, *wphi, *wo, *are, *aim, *bre, *bim, *cre, *cim, *logdt, *dskip, *gw;
  u16* out;
  char* ws;
};

DI unsigned pk2(float a, float b) { bf16x2 v; v[0] = (__bf16)a; v[1] = (__bf16)b; return __builtin_bit_cast(unsigned, v); }
DI float bf2f(u16 h) { return __uint_as_float((unsigned)h << 16); }
DI bf16x8 as_frag(u32x4 v) { return __builtin_bit_cast(bf16x8, v); }
DI bf16x8 pack8(const f32x16& a, int s) {
  u32x4 p;
  p[0] = pk2(a[8 * s + 0], a[8 * s + 1]); p[1] = pk2(a[8 * s + 2], a[8 * s + 3]);
  p[2] = pk2(a[8 * s + 4], a[8 * s + 5]); p[3] = pk2(a[8 * s + 6], a[8 * s + 7]);
  return as_frag(p);
}
DI void dma16(const void* sbase, unsigned voff, unsigned lds) {
  asm volatile("s_mov_b32 m0, %2\n\ts_nop 0\n\tglobal_load_lds_dwordx4 %0, %1" ::"v"(voff), "s"(sbase), "s"(lds) : "memory");
}
DI void dma16_sc1(const void* sbase, unsigned voff, unsigned lds) {
  asm volatile("s_mov_b32 m0, %2\n\ts_nop 0\n\tglobal_load_lds_dwordx4 %0, %1 sc1" ::"v"(voff), "s"(sbase), "s"(lds) : "memory");
}
DI void dma4(const void* sbase, unsigned voff, unsigned lds) {
  asm volatile("s_mov_b32 m0, %2\n\ts_nop 0\n\tglobal_load_lds_dword %0, %1" ::"v"(voff), "s"(sbase), "s"(lds) : "memory");
}
template <int N> DI void wait_vm_bar() { asm volatile("s_waitcnt vmcnt(%0) lgkmcnt(0)\n\ts_barrier" ::"n"(N) : "memory"); }
DI void bar_lgkm() { asm volatile("s_waitcnt lgkmcnt(0)\n\ts_barrier" ::: "memory"); }
DI bf16x8 lds_frag(unsigned a) { return *(LAS bf16x8*)a; }
DI bf16x4 lds_tr(unsigned a) { return __builtin_amdgcn_ds_read_tr16_b64_v4bf16((LAS bf16x4*)a); }
DI bf16x8 lds_tr2(unsigned a0, unsigned a1) { return __builtin_shufflevector(lds_tr(a0), lds_tr(a1), 0, 1, 2, 3, 4, 5, 6, 7); }
#define MFMA32(a, b, c) __builtin_amdgcn_mfma_f32_32x32x16_bf16((a), (b), (c), 0, 0, 0)
#define MFMA16(a, b, c) __builtin_amdgcn_mfma_f32_16x16x32_bf16((a), (b), (c), 0, 0, 0)
DI float fsum32(float a) {
  auto r = __builtin_amdgcn_permlane32_swap(__float_as_uint(a), __float_as_uint(a), false, false);
  unsigned r0 = r[0], r1 = r[1];
  return __uint_as_float(r0) + __uint_as_float(r1);
}
DI float fmax32(float a) {
  auto r = __builtin_amdgcn_permlane32_swap(__float_as_uint(a), __float_as_uint(a), false, false);
  unsigned r0 = r[0], r1 = r[1];
  return fmaxf(__uint_as_float(r0), __uint_as_float(r1));
}
DI float fmin32(float a) {
  auto r = __builtin_amdgcn_permlane32_swap(__float_as_uint(a), __float_as_uint(a), false, false);
  unsigned r0 = r[0], r1 = r[1];
  return fminf(__uint_as_float(r0), __uint_as_float(r1));
}
DI float fsum16(float a) {
  auto r = __builtin_amdgcn_permlane16_swap(__float_as_uint(a), __float_as_uint(a), false, false);
  unsigned r0 = r[0], r1 = r[1];
  return __uint_as_float(r0) + __uint_as_float(r1);
}
DI unsigned load_tag(const char* ws) {
  unsigned e;
  asm volatile("s_load_dword %0, %1, 0x0\n\ts_waitcnt lgkmcnt(0)" : "=s"(e) : "s"(ws) : "memory");
  return e;
}
DI unsigned mk_tag(unsigned e) { return (e + 1u) * 0x9E3779B1u + 0x7FEB352Du; }
typedef __amdgpu_buffer_rsrc_t rsrc_t;
DI u32x4 ld16_sc1(rsrc_t r, unsigned off) { return __builtin_amdgcn_raw_buffer_load_b128(r, off, 0, 16); }
DI u32x2 ld8_sc1(rsrc_t r, unsigned off) { return __builtin_amdgcn_raw_buffer_load_b64(r, off, 0, 16); }
DI unsigned ld4_sc1(rsrc_t r, unsigned off) { return __builtin_amdgcn_raw_buffer_load_b32(r, off, 0, 16); }
DI void st16_sc1(rsrc_t r, unsigned off, u32x4 v) { __builtin_amdgcn_raw_buffer_store_b128(v, r, off, 0, 16); }
DI void st8_sc1(rsrc_t r, unsigned off, u32x2 v) { __builtin_amdgcn_raw_buffer_store_b64(v, r, off, 0, 16); }
DI void st4_sc1(rsrc_t r, unsigned off, unsigned v) { __builtin_amdgcn_raw_buffer_store_b32(v, r, off, 0, 16); }
DI void wait_words(rsrc_t r, unsigned off, unsigned tag) {
#pragma unroll 1
  for (int sp = 0; sp < SPIN_CAP; ++sp) {
    asm volatile("" ::: "memory");
    unsigned f = ld4_sc1(r, off);
    if (__all(f == tag)) break;
    __builtin_amdgcn_s_sleep(2);
  }
  asm volatile("" ::: "memory");
}

#define STAMP(k)
#define STAMP_DECL
#define STAMP_FLUSH()

DI void dual_softmax(const f32x16& P, int e, bf16x8& fp, bf16x8& fm) {
  const float L = 1.44269504088896341f;
  float v[8];
#pragma unroll
  for (int j = 0; j < 8; ++j) v[j] = P[8 * e + j] * L;
  float mx = fmaxf(fmaxf(fmaxf(v[0], v[1]), fmaxf(v[2], v[3])), fmaxf(fmaxf(v[4], v[5]), fmaxf(v[6], v[7])));
  float mn = fminf(fminf(fminf(v[0], v[1]), fminf(v[2], v[3])), fminf(fminf(v[4], v[5]), fminf(v[6], v[7])));
  mx = fmax32(mx); mn = fmin32(mn);
  float a[8], b[8];
#pragma unroll
  for (int j = 0; j < 8; ++j) { a[j] = __builtin_amdgcn_exp2f(v[j] - mx); b[j] = __builtin_amdgcn_exp2f(mn - v[j]); }
  float sa = ((a[0] + a[1]) + (a[2] + a[3])) + ((a[4] + a[5]) + (a[6] + a[7]));
  float sb = ((b[0] + b[1]) + (b[2] + b[3])) + ((b[4] + b[5]) + (b[6] + b[7]));
  float ra = __builtin_amdgcn_rcpf(fsum32(sa)), rb = __builtin_amdgcn_rcpf(fsum32(sb));
  u32x4 x, y;
#pragma unroll
  for (int j = 0; j < 4; ++j) { x[j] = pk2(a[2 * j] * ra, a[2 * j + 1] * ra); y[j] = pk2(b[2 * j] * rb, b[2 * j + 1] * rb); }
  fp = as_frag(x); fm = as_frag(y);
}

DI void final_tile(const Params& p, rsrc_t ws, int ti, int q, int w, int lane, float gate, bool sc1) {
  const int i16 = lane & 15, g = lane >> 4, qq = i16 >> 2, pp = lane & 3;
  {
    const char* ysrc = p.ws + WS_Y + (unsigned)(ti * 512 + 64 * q + 16 * w) * 128u;
    const int rr = lane >> 3;
    const unsigned vo = rr * 128 + (((lane & 7) ^ (((rr >> 1) & 3) << 1)) << 4);
    dma16_sc1(ysrc, vo, A_Y + w * 2048);
    dma16_sc1(ysrc + 1024, vo, A_Y + w * 2048 + 1024);
  }
  u32x4 of[16];
  const unsigned obase = WS_O + (unsigned)(4 * ti + w) * 16384u + lane * 16;
#pragma unroll
  for (int k = 0; k < 16; ++k) of[k] = ld16_sc1(ws, obase + k * 1024);
  const int key = (2 * (g & 1) + (qq >> 1)) & 3;
  unsigned abase[4];
#pragma unroll
  for (int ct = 0; ct < 4; ++ct)
    abase[ct] = A_WO + (16 * (g >> 1) + 4 * (g & 1) + qq) * 128 + (((2 * ct + (pp >> 1)) ^ (key << 1)) << 4) + 8 * (pp & 1);
  f32x4 acc[4];
#pragma unroll
  for (int ct = 0; ct < 4; ++ct) acc[ct] = f32x4{0.f, 0.f, 0.f, 0.f};
#pragma unroll
  for (int k = 0; k < 16; ++k) {
#pragma unroll
    for (int ct = 0; ct < 4; ++ct) {
      bf16x8 a = lds_tr2(abase[ct] + k * 4096, abase[ct] + k * 4096 + 1024);
      acc[ct] = MFMA16(a, as_frag(of[k]), acc[ct]);
    }
  }
  wait_vm_bar<0>();
  const int ykey = (2 * g + (qq >> 1)) & 3;
  const unsigned ybase = A_Y + (4 * g + qq) * 128 + (((2 * w + (pp >> 1)) ^ (ykey << 1)) << 4) + 8 * (pp & 1);
  unsigned o2[4][2];
#pragma unroll
  for (int ct = 0; ct < 4; ++ct) {
    bf16x4 yb = lds_tr(ybase + ct * 2048);
    float r[4];
#pragma unroll
    for (int j = 0; j < 4; ++j) { float b = (float)yb[j]; r[j] = b + gate * (acc[ct][j] - b); }
    o2[ct][0] = pk2(r[0], r[1]); o2[ct][1] = pk2(r[2], r[3]);
  }
  char* orow = (char*)p.out + (unsigned)(64 * ti + 16 * w + i16) * 1024u + 128 * q + 32 * (g & 1) + 16 * (g >> 1);
#pragma unroll
  for (int c2 = 0; c2 < 2; ++c2) {
    auto s0 = __builtin_amdgcn_permlane16_swap(o2[2 * c2][0], o2[2 * c2 + 1][0], false, false);
    auto s1 = __builtin_amdgcn_permlane16_swap(o2[2 * c2][1], o2[2 * c2 + 1][1], false, false);
    u32x4 v; v[0] = s0[0]; v[1] = s1[0]; v[2] = s0[1]; v[3] = s1[1];
    *(u32x4*)(orow + 64 * c2) = v;
  }
}

DI void wo_dma(const Params& p, int q, int part, int nparts, int lane) {
  const int rr = lane >> 3;
  const unsigned vo = rr * 1024 + (((lane & 7) ^ (((rr >> 1) & 3) << 1)) << 4);
  const int n = 64 / nparts;
  const char* src = (const char*)p.wo + 128 * q + (unsigned)(part * n) * 8192u;
  unsigned dst = A_WO + part * n * 1024;
#pragma unroll 4
  for (int i = 0; i < n; ++i) dma16(src + i * 8192, vo, dst + i * 1024);
}

DI void att_block(const Params& p, int b) {
  STAMP_DECL;
  STAMP(0);
  const int tid = threadIdx.x, lane = tid & 63, w = __builtin_amdgcn_readfirstlane(tid >> 6);
  const int xcd = b & 7, bi = b >> 3, ti = 4 * xcd + (bi & 3), q = bi >> 2;
  const int th = w & 1, hp = w >> 1;
  const int r = lane & 31, h = lane >> 5, blk = (lane >> 4) & 1, i16 = lane & 15, qq = i16 >> 2, pp = lane & 3;
  __builtin_amdgcn_s_setprio(2);
  const int rr = lane >> 3, sl = lane & 7;
  const unsigned vx0 = rr * 1024 + ((sl ^ (rr >> 1)) << 4), vx1 = rr * 1024 + ((sl ^ (4 + (rr >> 1))) << 4);
  const unsigned vw = rr * 3072 + ((sl ^ (((rr >> 1) & 1) << 2)) << 4);
  const char* xs = (const char*)p.x + (unsigned)(64 * ti + 16 * w) * 1024u;
  const char* wsrc = (const char*)p.wqkv + (unsigned)(16 * w) * 3072u + 128 * q;
  const unsigned ldw = w * 2048;
  auto stage_kv = [&](int c, unsigned slot) {
    dma16(xs + c * 128, vx0, slot + ldw);
    dma16(xs + c * 128 + 8192, vx1, slot + ldw + 1024);
    dma16(wsrc + c * 196608 + 1024, vw, slot + 8192 + ldw);
    dma16(wsrc + c * 196608 + 1024 + 24576, vw, slot + 8192 + ldw + 1024);
    dma16(wsrc + c * 196608 + 2048, vw, slot + 16384 + ldw);
    dma16(wsrc + c * 196608 + 2048 + 24576, vw, slot + 16384 + ldw + 1024);
  };
  auto stage_q = [&](int c0, int n, unsigned slot) {
    for (int i = 0; i < n; ++i) {
      dma16(wsrc + (c0 + i) * 196608, vw, slot + i * 8192 + ldw);
      dma16(wsrc + (c0 + i) * 196608 + 24576, vw, slot + i * 8192 + ldw + 1024);
    }
  };
  if (w < 2) dma16((const char*)p.wphi + q * 2048 + w * 1024, lane * 16, A_WPHI + w * 1024);
  else if (w == 2) dma16((const char*)p.gw, lane * 16, A_GW);
  stage_kv(0, 0); stage_kv(1, SLOT); stage_kv(2, 2 * SLOT);
  {
    const unsigned T = bi * 256 + tid;
    unsigned c = T / 1280u, j = T - c * 1280u;
    c += 3;
    const char* a0 = (j < 256) ? (const char*)p.x + (256 * xcd + j) * 1024u + c * 128
                               : (const char*)p.wqkv + (64 * c + ((j - 256) >> 4)) * 3072u + 1024 + ((j - 256) & 15) * 128;
    if (T >= 6400) a0 = (const char*)p.wqkv;
    const char* a1 = (T < 4096) ? (const char*)p.wqkv + (T >> 3) * 3072u + (T & 7) * 128 : (const char*)p.wo + (T - 4096) * 128u;
    asm volatile("s_mov_b32 m0, %2\n\ts_nop 0\n\tglobal_load_lds_dword %0, off\n\tglobal_load_lds_dword %1, off" ::"v"(a0), "v"(a1), "s"(A_DUMMY) : "memory");
  }
  constexpr int NW = 2;
  const unsigned tag = mk_tag(load_tag(p.ws + WS_E));
  const rsrc_t ws = __builtin_amdgcn_make_buffer_rsrc(p.ws, 0, WS_END, 0x00020000);

  unsigned xa[4];
#pragma unroll
  for (int s = 0; s < 4; ++s) xa[s] = (32 * th + r) * 128 + (((2 * s + h) ^ ((r >> 1) & 7)) << 4);
  const unsigned wa = (8 * h + qq) * 128 + (((4 * hp + 2 * blk + (pp >> 1)) ^ ((qq >> 1) << 2)) << 4) + 8 * (pp & 1);

  bf16x8 X[32];
  f32x16 ak, av;
#pragma unroll
  for (int i = 0; i < 16; ++i) { ak[i] = 0.f; av[i] = 0.f; }
#pragma unroll
  for (int c = 0; c < 8; ++c) {
    const unsigned slot = (c % 3) * SLOT;
    if (c < 3) wait_vm_bar<12 + NW>(); else wait_vm_bar<12>();
    if (c == 0) STAMP(1);
    bf16x8 wk[4], wv[4];
#pragma unroll
    for (int s = 0; s < 4; ++s) {
      X[4 * c + s] = lds_frag(slot + xa[s]);
      wk[s] = lds_tr2(slot + 8192 + wa + s * 2048, slot + 8192 + wa + s * 2048 + 512);
      wv[s] = lds_tr2(slot + 16384 + wa + s * 2048, slot + 16384 + wa + s * 2048 + 512);
    }
    bar_lgkm();
    if (c < 5) stage_kv(c + 3, slot);
    else if (c == 5) stage_q(0, 3, slot);
    else if (c == 6) stage_q(3, 3, slot);
    else stage_q(6, 2, slot);
#pragma unroll
    for (int s = 0; s < 4; ++s) {
      ak = MFMA32(wk[s], X[4 * c + s], ak);
      av = MFMA32(X[4 * c + s], wv[s], av);
    }
  }
  STAMP(2);
  bf16x8 A1, A2, SEL0, SEL1;
  {
    const unsigned a = A_WPHI + (2 * hp + blk) * 512 + (4 * h + qq) * 32 + 8 * pp;
    u32x4 wf = __builtin_bit_cast(u32x4, lds_tr2(a, a + 256));
    u32x4 z = {0u, 0u, 0u, 0u};
    A1 = as_frag(blk ? z : wf);
    A2 = as_frag(blk ? wf : z);
    u32x4 s0, s1;
#pragma unroll
    for (int d = 0; d < 4; ++d) {
      const int f0 = ((2 * d) & 3) + 8 * (d >> 1) + 4 * h;
      s0[d] = (r == f0 ? 0x3F80u : 0u) | (r == f0 + 1 ? 0x3F800000u : 0u);
      s1[d] = (r == f0 + 16 ? 0x3F80u : 0u) | (r == f0 + 17 ? 0x3F800000u : 0u);
    }
    SEL0 = as_frag(s0); SEL1 = as_frag(s1);
  }
  f32x16 zero16;
#pragma unroll
  for (int i = 0; i < 16; ++i) zero16[i] = 0.f;
  f32x16 R, Z;
  {
    f32x16 P = MFMA32(A1, pack8(ak, 0), zero16);
    P = MFMA32(A2, pack8(ak, 1), P);
    const u32x4 ones = {0x3F803F80u, 0x3F803F80u, 0x3F803F80u, 0x3F803F80u};
    const u32x4 v0 = __builtin_bit_cast(u32x4, pack8(av, 0)), v1 = __builtin_bit_cast(u32x4, pack8(av, 1));
    f32x16 Dh[2];
#pragma unroll
    for (int e = 0; e < 2; ++e) {
      bf16x8 fp, fm;
      dual_softmax(P, e, fp, fm);
      f32x16 T = MFMA32(fp, SEL0, zero16);
      T = MFMA32(fm, SEL1, T);
      const bool mine = (blk == e);
      Dh[e] = MFMA32(pack8(T, 0), as_frag(mine ? v0 : ones), zero16);
      Dh[e] = MFMA32(pack8(T, 1), as_frag(mine ? v1 : ones), Dh[e]);
    }
#pragma unroll
    for (int i = 0; i < 16; ++i) { R[i] = blk ? Dh[1][i] : Dh[0][i]; Z[i] = blk ? Dh[0][i] : Dh[1][i]; }
  }
  {
    float kr[8], kz[8];
    f32x4 s0, s1, z0, z1;
#pragma unroll
    for (int j = 0; j < 4; ++j) {
      s0[j] = th ? R[j] : R[8 + j]; s1[j] = th ? R[4 + j] : R[12 + j];
      z0[j] = th ? Z[j] : Z[8 + j]; z1[j] = th ? Z[4 + j] : Z[12 + j];
      kr[j] = th ? R[8 + j] : R[j]; kr[4 + j] = th ? R[12 + j] : R[4 + j];
      kz[j] = th ? Z[8 + j] : Z[j]; kz[4 + j] = th ? Z[12 + j] : Z[4 + j];
    }
    *(LAS f32x4*)(A_XCH + w * 2048 + lane * 16) = s0;
    *(LAS f32x4*)(A_XCH + w * 2048 + 1024 + lane * 16) = s1;
    if (i16 == 0) {
      *(LAS f32x4*)(A_XZ + w * 128 + (lane >> 4) * 32) = z0;
      *(LAS f32x4*)(A_XZ + w * 128 + (lane >> 4) * 32 + 16) = z1;
    }
    wait_vm_bar<0>();
    const f32x4 t0 = *(LAS f32x4*)(A_XCH + (w ^ 1) * 2048 + lane * 16), t1 = *(LAS f32x4*)(A_XCH + (w ^ 1) * 2048 + 1024 + lane * 16);
    const f32x4 y0 = *(LAS f32x4*)(A_XZ + (w ^ 1) * 128 + (lane >> 4) * 32), y1 = *(LAS f32x4*)(A_XZ + (w ^ 1) * 128 + (lane >> 4) * 32 + 16);
    const unsigned slk = (((2 * hp + blk) * 2 + th) * 2) * 2 + h;
    const unsigned slz = (((2 * hp + 1 - blk) * 2 + th) * 2) * 2 + h;
    const unsigned ok = WS_L1K + ((q * 32 + slk) * 32 + ti) * 256 + i16 * 16;
    st16_sc1(ws, ok, u32x4{pk2(kr[0] + t0[0], kr[1] + t0[1]), tag, pk2(kr[2] + t0[2], kr[3] + t0[3]), tag});
    st16_sc1(ws, ok + 2 * 32 * 256, u32x4{pk2(kr[4] + t1[0], kr[5] + t1[1]), tag, pk2(kr[6] + t1[2], kr[7] + t1[3]), tag});
    if (i16 == 0) {
      const unsigned oz = WS_L1Z + ((q * 32 + slz) * 32 + ti) * 16;
      st16_sc1(ws, oz, u32x4{pk2(kz[0] + y0[0], kz[1] + y0[1]), tag, pk2(kz[2] + y0[2], kz[3] + y0[3]), tag});
      st16_sc1(ws, oz + 2 * 32 * 16, u32x4{pk2(kz[4] + y1[0], kz[5] + y1[1]), tag, pk2(kz[6] + y1[2], kz[7] + y1[3]), tag});
    }
  }
  STAMP(3);
  bf16x8 pq[2][2];
  {
    f32x16 aq = zero16;
#pragma unroll
    for (int c = 0; c < 8; ++c) {
      const unsigned base = (c < 3 ? 2 * SLOT + c * 8192 : c < 6 ? (c - 3) * 8192 : SLOT + (c - 6) * 8192) + wa;
#pragma unroll
      for (int s = 0; s < 4; ++s) aq = MFMA32(lds_tr2(base + s * 2048, base + s * 2048 + 512), X[4 * c + s], aq);
    }
    bar_lgkm();
    STAMP(4);
    if (w >= 2) wo_dma(p, q, w - 2, 2, lane);
    f32x16 P = MFMA32(A1, pack8(aq, 0), zero16);
    P = MFMA32(A2, pack8(aq, 1), P);
    dual_softmax(P, 0, pq[0][0], pq[0][1]);
    dual_softmax(P, 1, pq[1][0], pq[1][1]);
  }
  STAMP(5);
  unsigned lo_bits = 0x00003F80u, hi_bits = 0x3F800000u;
  asm volatile("" : "+s"(lo_bits), "+s"(hi_bits));
  const bf16x2 LO = __builtin_bit_cast(bf16x2, lo_bits), HI = __builtin_bit_cast(bf16x2, hi_bits);
  if (w == 0) {
    const unsigned o = WS_L1K + ((q * 32 + ti) * 32 + (lane >> 4) * 8) * 256 + i16 * 16;
    u32x4 g[8];
#pragma unroll 1
  for (int sp = 0; sp < SPIN_CAP; ++sp) {
      asm volatile("" ::: "memory");
      bool ok = true;
#pragma unroll
      for (int i = 0; i < 8; ++i) g[i] = ld16_sc1(ws, o + i * 256);
#pragma unroll
      for (int i = 0; i < 8; ++i) ok &= (g[i][1] == tag) & (g[i][3] == tag);
      if (__all(ok)) break;
      __builtin_amdgcn_s_sleep(2);
    }
    float a = 0.f, bb = 0.f, c = 0.f, d = 0.f;
#pragma unroll
    for (int i = 0; i < 8; ++i) {
      const unsigned p0 = g[i][0], p1 = g[i][2];
      a = __builtin_amdgcn_fdot2_f32_bf16(__builtin_bit_cast(bf16x2, p0), LO, a, false);
      bb = __builtin_amdgcn_fdot2_f32_bf16(__builtin_bit_cast(bf16x2, p0), HI, bb, false);
      c = __builtin_amdgcn_fdot2_f32_bf16(__builtin_bit_cast(bf16x2, p1), LO, c, false);
      d = __builtin_amdgcn_fdot2_f32_bf16(__builtin_bit_cast(bf16x2, p1), HI, d, false);
    }
    auto ac = __builtin_amdgcn_permlane32_swap(__float_as_uint(a), __float_as_uint(c), false, false);
    auto bd = __builtin_amdgcn_permlane32_swap(__float_as_uint(bb), __float_as_uint(d), false, false);
    const unsigned ac0 = ac[0], ac1 = ac[1], bd0 = bd[0], bd1 = bd[1];
    const float s1 = fsum16(__uint_as_float(ac0) + __uint_as_float(ac1)), s2 = fsum16(__uint_as_float(bd0) + __uint_as_float(bd1));
    if (blk == 0) st8_sc1(ws, WS_L2 + (q * 32 + ti) * 272 + i16 * 16 + 8 * h, u32x2{pk2(s1, s2), tag});
  } else if (w == 1) {
    const unsigned o = WS_L1Z + ((q * 32 + ti) * 32 + r) * 16;
    u32x4 g;
#pragma unroll 1
  for (int sp = 0; sp < SPIN_CAP; ++sp) {
      asm volatile("" ::: "memory");
      g = ld16_sc1(ws, o);
      if (__all((g[1] == tag) & (g[3] == tag))) break;
      __builtin_amdgcn_s_sleep(2);
    }
    const unsigned pw = h ? g[2] : g[0];
    float a = __uint_as_float(pw << 16), bb = __uint_as_float(pw & 0xFFFF0000u);
#pragma unroll
    for (int m = 1; m < 16; m <<= 1) { a += __shfl_xor(a, m); bb += __shfl_xor(bb, m); }
    a = fsum16(a); bb = fsum16(bb);
    if (r == 0) st8_sc1(ws, WS_L2 + (q * 32 + ti) * 272 + 256 + 8 * h, u32x2{pk2(a, bb), tag});
  }
  STAMP(6);
  {
    float ga = 0.f, gb = 0.f;
    const unsigned gaddr = A_GW + hp * 512 + 16 * h;
#pragma unroll
    for (int s = 0; s < 16; ++s) {
      const u32x4 gv = *(LAS u32x4*)(gaddr + s * 32);
      const u32x4 xv = __builtin_bit_cast(u32x4, hp ? X[16 + s] : X[s]);
#pragma unroll
      for (int d = 0; d < 4; d += 2) {
        const unsigned x0 = xv[d], x1 = xv[d + 1], g0 = gv[d], g1 = gv[d + 1];
        ga = __builtin_amdgcn_fdot2_f32_bf16(__builtin_bit_cast(bf16x2, x0), __builtin_bit_cast(bf16x2, g0), ga, false);
        gb = __builtin_amdgcn_fdot2_f32_bf16(__builtin_bit_cast(bf16x2, x1), __builtin_bit_cast(bf16x2, g1), gb, false);
      }
    }
    *(LAS float*)(A_GP + (32 * th + r) * 16 + (2 * hp + h) * 4) = ga + gb;
  }
  STAMP(7);
  {
    const unsigned o = WS_L2 + (q * 32 + hp * 16 + h) * 272 + (r < 16 ? r : 16) * 16;
    u32x4 g[8];
#pragma unroll 1
  for (int sp = 0; sp < SPIN_CAP; ++sp) {
      asm volatile("" ::: "memory");
      bool ok = true;
#pragma unroll
      for (int i = 0; i < 8; ++i) g[i] = ld16_sc1(ws, o + i * 544);
#pragma unroll
      for (int i = 0; i < 8; ++i) ok &= (g[i][1] == tag) & (g[i][3] == tag);
      if (__all(ok)) break;
      __builtin_amdgcn_s_sleep(2);
    }
    STAMP(8);
    const unsigned oo = WS_O + ((4 * ti + 2 * th + blk) * 16 + 2 * q + hp) * 1024 + (i16 + 16 * h) * 16;
#pragma unroll
    for (int e = 0; e < 2; ++e) {
      const u32x4 f0 = {g[4 * e][0], g[4 * e][2], g[4 * e + 1][0], g[4 * e + 1][2]};
      const u32x4 f1 = {g[4 * e + 2][0], g[4 * e + 2][2], g[4 * e + 3][0], g[4 * e + 3][2]};
      f32x16 Dn = MFMA32(as_frag(f0), pq[e][0], zero16);
      Dn = MFMA32(as_frag(f1), pq[e][1], Dn);
      const float rd = __builtin_amdgcn_rcpf(Dn[8] + 1e-6f);
      const u32x4 ov = {pk2(Dn[0] * rd, Dn[1] * rd), pk2(Dn[2] * rd, Dn[3] * rd), pk2(Dn[4] * rd, Dn[5] * rd), pk2(Dn[6] * rd, Dn[7] * rd)};
      st16_sc1(ws, oo + e * 512, ov);
    }
  }
  wait_vm_bar<0>();
  if (tid == 0) st4_sc1(ws, WS_FO + (ti * 8 + q) * FS, tag);
  STAMP(9);
  const f32x4 gp = *(LAS f32x4*)(A_GP + (16 * w + i16) * 16);
  const float gate = __builtin_amdgcn_rcpf(1.f + __builtin_amdgcn_exp2f(-1.44269504088896341f * ((gp[0] + gp[1]) + (gp[2] + gp[3]))));
  wait_words(ws, lane < 32 ? WS_FY + (q * 32 + lane) * FS : WS_FO + (ti * 8 + (lane & 7)) * FS, tag);
  STAMP(10);
  final_tile(p, ws, ti, q, w, lane, gate, true);
  STAMP(11);
  STAMP_FLUSH();
}

DI f32x2 cmul(f32x2 a, f32x2 b, f32x2 bn) {
  f32x2 ax = {a[0], a[0]}, ay = {a[1], a[1]};
  return __builtin_elementwise_fma(ay, bn, ax * b);
}
DI f32x2 rot(f32x2 b) { return f32x2{-b[1], b[0]}; }
DI f32x2 cexp_l(float lr2, float li2, float l) {
  const float m = __builtin_amdgcn_exp2f(lr2 * l), ph = __builtin_amdgcn_fractf(li2 * l);
  return f32x2{m * __builtin_amdgcn_cosf(ph), m * __builtin_amdgcn_sinf(ph)};
}
DI void put(unsigned a, f32x2 v) { *(LAS unsigned*)a = pk2(v[0], v[1]); }

DI void ssm_block(const Params& p, int sidx) {
  STAMP_DECL;
  STAMP(0);
  const int tid = threadIdx.x, lane = tid & 63, w = __builtin_amdgcn_readfirstlane(tid >> 6);
  const int xcd = sidx & 7, bi = sidx >> 3;
  const int i16 = lane & 15, g = lane >> 4, qq = i16 >> 2, pp = lane & 3;
  const int chl = w >> 1, ch = 64 * xcd + 2 * bi + chl;
  {
    const char* xs = (const char*)p.x + (unsigned)(64 * bi + 16 * w) * 1024u + 128 * xcd;
    const unsigned vo = (lane >> 3) * 1024 + (lane & 7) * 16;
    dma16(xs, vo, S_XT + w * 2048);
    dma16(xs + 8192, vo, S_XT + w * 2048 + 1024);
  }
  const int n = tid & 127, pi = ch * 128 + n;
  const float are = bf2f(p.are[pi]), aim = bf2f(p.aim[pi]), bre = bf2f(p.bre[pi]), bim = bf2f(p.bim[pi]);
  const float cre = bf2f(p.cre[pi]), cim = bf2f(p.cim[pi]);
  const float ldt = bf2f(p.logdt[ch]), dsk = bf2f(p.dskip[ch]);
  const unsigned epoch = load_tag(p.ws + WS_E);
  const unsigned tag = mk_tag(epoch);
  const rsrc_t ws = __builtin_amdgcn_make_buffer_rsrc(p.ws, 0, WS_END, 0x00020000);
  wait_vm_bar<0>();
  STAMP(1);
  {
    const unsigned a = S_XT + (8 * g + qq) * 128 + (16 * w + 4 * pp) * 2;
    const unsigned o = WS_XT + (64 * xcd + 16 * w + i16) * 4096 + (64 * bi + 8 * g) * 2;
    st16_sc1(ws, o, __builtin_bit_cast(u32x4, lds_tr2(a, a + 512)));
    st16_sc1(ws, o + 64, __builtin_bit_cast(u32x4, lds_tr2(a + 4096, a + 4096 + 512)));
  }
  {
    const float dt = (float)(__bf16)expf(ldt);
    const float lr2 = dt * are * 1.44269504088896341f, li2 = dt * aim * 0.15915494309189535f;
    const f32x2 dB = {dt * bre, dt * bim};
    const f32x2 W = cmul(f32x2{cre, cim}, dB, rot(dB));
    const f32x2 E1 = cexp_l(lr2, li2, 1.f), E8 = cexp_l(lr2, li2, 8.f), G = cexp_l(lr2, li2, 256.f);
    const unsigned tx = chl * 2 * TSZ + n * 4, ty = tx + TSZ;
    const f32x2 E1n = rot(E1), Gn = rot(G);
    const f32x2 E2 = cmul(E1, E1, E1n), E2n = rot(E2);
    const f32x2 X1 = cmul(W, E1, E1n), X2 = cmul(W, E2, E2n), X3 = cmul(X1, E2, E2n), X4 = cmul(X2, E2, E2n);
    const f32x2 X5 = cmul(X3, E2, E2n), X6 = cmul(X4, E2, E2n), X7 = cmul(X5, E2, E2n), X8 = cmul(W, E8, rot(E8));
    put(tx + 0 * TROW, W); put(tx + 1 * TROW, X1); put(tx + 2 * TROW, X2); put(tx + 3 * TROW, X3);
    put(tx + 4 * TROW, X4); put(tx + 5 * TROW, X5); put(tx + 6 * TROW, X6); put(tx + 7 * TROW, X7);
    f32x2 A = cmul(W, G, Gn), B = cmul(X8, G, Gn), C = cmul(X4, G, Gn);
    put(tx + 8 * TROW, A); put(tx + 9 * TROW, cmul(X2, G, Gn)); put(tx + 10 * TROW, C);
    put(tx + 12 * TROW, C); put(tx + 13 * TROW, cmul(X6, G, Gn)); put(tx + 14 * TROW, B);
    A = cmul(A, G, Gn); B = cmul(B, G, Gn); C = cmul(C, G, Gn);
    put(tx + 16 * TROW, A); put(tx + 17 * TROW, C); put(tx + 20 * TROW, C); put(tx + 21 * TROW, B);
    A = cmul(A, G, Gn); B = cmul(B, G, Gn); C = cmul(C, G, Gn);
    put(tx + 18 * TROW, A); put(tx + 19 * TROW, C); put(tx + 22 * TROW, C); put(tx + 23 * TROW, B);
#pragma unroll
    for (int m = 0; m < 4; ++m) {
      A = cmul(A, G, Gn); B = cmul(B, G, Gn);
      put(tx + (24 + m) * TROW, A); put(tx + (28 + m) * TROW, B);
    }
    const f32x2 F1 = {E8[0], -E8[1]}, F1n = rot(F1);
    const f32x2 F2 = cmul(F1, F1, F1n), F2n = rot(F2);
    const f32x2 F4 = cmul(F2, F2, F2n), F4n = rot(F4);
    f32x2 y0 = {1.f, 0.f}, y1 = F1, y2 = F2, y3 = cmul(F2, F1, F1n);
    put(ty + 0 * TROW, y0); put(ty + 1 * TROW, y1); put(ty + 2 * TROW, y2); put(ty + 3 * TROW, y3);
    y0 = F4;
#pragma unroll
    for (int m = 1; m < 8; ++m) {
      if (m > 1) y0 = cmul(y0, F4, F4n);
      y1 = cmul(y1, F4, F4n); y2 = cmul(y2, F4, F4n); y3 = cmul(y3, F4, F4n);
      put(ty + (4 * m) * TROW, y0); put(ty + (4 * m + 1) * TROW, y1); put(ty + (4 * m + 2) * TROW, y2); put(ty + (4 * m + 3) * TROW, y3);
    }
  }
  wait_vm_bar<0>();
  if (tid == 0) st4_sc1(ws, WS_FX + (xcd * 32 + bi) * FS, tag);
  STAMP(2);
  if (w < 2) {
    const int r = lane & 31, h = lane >> 5;
    const unsigned ta = w * 2 * TSZ + r * TROW + 16 * h;
    f32x16 v;
#pragma unroll
    for (int i = 0; i < 16; ++i) v[i] = 0.f;
#pragma unroll
    for (int s = 0; s < 16; ++s) v = MFMA32(lds_frag(ta + 32 * s), lds_frag(ta + TSZ + 32 * s), v);
    const unsigned ka = S_KR + w * KSZ + (2044 - 8 * r - 4 * h - 1792) * 2;
    *(LAS u32x2*)(ka + 2 * 1792) = u32x2{pk2(v[3], v[2]), pk2(v[1], v[0])};
    *(LAS u32x2*)(ka + 2 * 1536) = u32x2{pk2(v[6], v[5]), pk2(v[4], v[4])};
    *(LAS u32x2*)(ka + 2 * 1280) = u32x2{pk2(v[9], h ? v[9] : v[8]), pk2(v[8], v[8])};
    *(LAS u32x2*)(ka + 2 * 1024) = u32x2{pk2(v[11], h ? v[11] : v[10]), pk2(v[10], v[10])};
#pragma unroll
    for (int a = 0; a < 4; ++a) {
      const float t = v[12 + a];
      auto sw = __builtin_amdgcn_permlane32_swap(__float_as_uint(t), __float_as_uint(t), false, false);
      const unsigned lo = sw[0];
      const float first = (r & 1) ? t : __uint_as_float(lo);
      *(LAS u32x2*)(ka + 2 * (768 - 256 * a)) = u32x2{pk2(t, t), pk2(t, first)};
    }
  } else if (w == 2) {
    if (lane < 60) *(LAS u32x4*)(S_KR + (lane >= 30 ? KSZ - 480 : 0) + 4096 + lane * 16) = u32x4{0u, 0u, 0u, 0u};
    wait_words(ws, WS_FX + (xcd * 32 + (lane & 31)) * FS, tag);
  }
  bar_lgkm();
  STAMP(3);
  {
    const unsigned o = WS_XT + ch * 4096 + n * 32;
    const u32x4 a = ld16_sc1(ws, o), b = ld16_sc1(ws, o + 16);
    const u32x2 c = ld8_sc1(ws, o + 32);
    const unsigned d[10] = {a[0], a[1], a[2], a[3], b[0], b[1], b[2], b[3], c[0], c[1]};
    unsigned e[9];
#pragma unroll
    for (int k = 0; k < 9; ++k) e[k] = __builtin_amdgcn_alignbit(d[k + 1], d[k], 16);
    const unsigned u = chl * USZ + 32 + n * 32;
    *(LAS u32x4*)(u) = a; *(LAS u32x4*)(u + 16) = b;
    *(LAS u32x4*)(u + UCP) = u32x4{e[0], e[1], e[2], e[3]}; *(LAS u32x4*)(u + UCP + 16) = u32x4{e[4], e[5], e[6], e[7]};
    *(LAS u32x4*)(u + 2 * UCP) = u32x4{d[1], d[2], d[3], d[4]}; *(LAS u32x4*)(u + 2 * UCP + 16) = u32x4{d[5], d[6], d[7], d[8]};
    *(LAS u32x4*)(u + 3 * UCP) = u32x4{e[1], e[2], e[3], e[4]}; *(LAS u32x4*)(u + 3 * UCP + 16) = u32x4{e[5], e[6], e[7], e[8]};
    if (n == 0) {
      const u32x4 z = {0u, 0u, 0u, 0u};
      const unsigned u0 = chl * USZ;
      *(LAS u32x4*)(u0) = z; *(LAS u32x4*)(u0 + 16) = z;
      *(LAS u32x4*)(u0 + UCP) = z; *(LAS u32x4*)(u0 + UCP + 16) = u32x4{0u, 0u, 0u, d[0] << 16};
      *(LAS u32x4*)(u0 + 2 * UCP) = z; *(LAS u32x4*)(u0 + 2 * UCP + 16) = u32x4{0u, 0u, 0u, d[0]};
      *(LAS u32x4*)(u0 + 3 * UCP) = z; *(LAS u32x4*)(u0 + 3 * UCP + 16) = u32x4{0u, 0u, d[0] << 16, e[0]};
    }
  }
  bar_lgkm();
  STAMP(4);
  f32x4 acc[8];
#pragma unroll
  for (int c = 0; c < 8; ++c) acc[c] = f32x4{0.f, 0.f, 0.f, 0.f};
  {
    const int sg = (i16 + 1) & 3;
    unsigned ua = chl * USZ + sg * UCP + (i16 + 1 - sg + 8 * g) * 2 + (w & 1) * 256;
    unsigned kb = S_KR + chl * KSZ + (240 - 16 * i16 + 8 * g) * 2 + (w & 1) * 256;
#pragma unroll 1
    for (int rho = 0; rho < 4; ++rho) {
      bf16x8 A[8], G[8];
#pragma unroll
      for (int m = 0; m < 8; ++m) {
        const u32x2 lo = *(LAS u32x2*)(ua + 512 * m), hi = *(LAS u32x2*)(ua + 512 * m + 8);
        A[m] = as_frag(u32x4{lo[0], lo[1], hi[0], hi[1]});
        G[m] = lds_frag(kb + 512 * (7 - m));
      }
#pragma unroll
      for (int c = 0; c < 8; ++c)
#pragma unroll
        for (int m = 0; m <= c; ++m) acc[c] = MFMA16(A[m], G[c - m], acc[c]);
      ua += 64; kb += 64;
    }
  }
  STAMP(5);
  {
    f32x4 keep[4];
    const unsigned ro = S_RED + w * 4096 + lane * 16, ri = S_RED + (w ^ 1) * 4096 + lane * 16;
    if (w & 1) {
#pragma unroll
      for (int k = 0; k < 4; ++k) { *(LAS f32x4*)(ro + k * 1024) = acc[k]; keep[k] = acc[4 + k]; }
    } else {
#pragma unroll
      for (int k = 0; k < 4; ++k) { *(LAS f32x4*)(ro + k * 1024) = acc[4 + k]; keep[k] = acc[k]; }
    }
    bar_lgkm();
    const int c0 = 4 * (w & 1);
    unsigned o2[4][2];
#pragma unroll
    for (int k = 0; k < 4; ++k) {
      const f32x4 t = *(LAS f32x4*)(ri + k * 1024);
      const u32x2 uu = *(LAS u32x2*)(chl * USZ + (256 * (c0 + k) + 16 * i16 + 4 * g + 16) * 2);
      const unsigned u0 = uu[0], u1 = uu[1];
      o2[k][0] = pk2(keep[k][0] + t[0] + dsk * __uint_as_float(u0 << 16), keep[k][1] + t[1] + dsk * __uint_as_float(u0 & 0xFFFF0000u));
      o2[k][1] = pk2(keep[k][2] + t[2] + dsk * __uint_as_float(u1 << 16), keep[k][3] + t[3] + dsk * __uint_as_float(u1 & 0xFFFF0000u));
    }
#pragma unroll
    for (int k = 0; k < 2; ++k) {
      auto s0 = __builtin_amdgcn_permlane16_swap(o2[2 * k][0], o2[2 * k + 1][0], false, false);
      auto s1 = __builtin_amdgcn_permlane16_swap(o2[2 * k][1], o2[2 * k + 1][1], false, false);
      u32x4 v; v[0] = s0[0]; v[1] = s1[0]; v[2] = s0[1]; v[3] = s1[1];
      const int t = 256 * (c0 + 2 * k + (g & 1)) + 16 * i16 + 8 * (g >> 1);
      st16_sc1(ws, WS_Y + ((t >> 6) * 512 + ch) * 128 + (t & 63) * 2, v);
    }
  }
  wait_vm_bar<0>();
  if (tid == 0) st4_sc1(ws, WS_FY + (xcd * 32 + bi) * FS, tag);
  STAMP(6);
  if (sidx == 0 && w == 0) {
#pragma unroll 1
    for (int k = 0; k < 4; ++k) wait_words(ws, WS_FX + (k * 64 + lane) * FS, tag);
    wait_words(ws, WS_L2 + (lane & 7) * 32 * 272 + 4, tag);
    if (lane == 0) st4_sc1(ws, WS_E, epoch + 1u);
  }
  STAMP(7);
  STAMP_FLUSH();
}

__global__ void __launch_bounds__(256, 2) hh_s4_mix_kernel(Params p) {
  if (blockIdx.x < 256) att_block(p, blockIdx.x);
  else ssm_block(p, blockIdx.x - 256);
}

extern "C" void kernel_launch(void* const* d_in, const int* in_sizes, int n_in, void* d_out, int out_size, void* d_ws,
                              size_t ws_size, hipStream_t stream) {
  Params p;
  p.x = (const u16*)d_in[0]; p.wqkv = (const u16*)d_in[1]; p.wphi = (const u16*)d_in[2]; p.wo = (const u16*)d_in[3];
  p.are = (const u16*)d_in[5]; p.aim = (const u16*)d_in[6]; p.bre = (const u16*)d_in[7]; p.bim = (const u16*)d_in[8];
  p.cre = (const u16*)d_in[9]; p.cim = (const u16*)d_in[10]; p.logdt = (const u16*)d_in[11]; p.dskip = (const u16*)d_in[12];
  p.gw = (const u16*)d_in[13];
  p.out = (u16*)d_out;
  p.ws = (char*)d_ws;
  (void)hipFuncSetAttribute((const void*)hh_s4_mix_kernel, hipFuncAttributeMaxDynamicSharedMemorySize, LDS_BYTES);
  hh_s4_mix_kernel<<<512, 256, LDS_BYTES, stream>>>(p);
}
```

```cpp
#include <hip/hip_runtime.h>
#include <hip/hip_bf16.h>
#include <stdint.h>
#pragma clang diagnostic ignored "-Wint-to-pointer-cast"

#define LAS __attribute__((address_space(3)))
#define GAS __attribute__((address_space(1)))
#define DI __device__ __forceinline__
typedef __bf16 bf16x8 __attribute__((ext_vector_type(8)));
typedef __bf16 bf16x4 __attribute__((ext_vector_type(4)));
typedef __bf16 bf16x2 __attribute__((ext_vector_type(2)));
typedef float f32x16 __attribute__((ext_vector_type(16)));
typedef float f32x4 __attribute__((ext_vector_type(4)));
typedef float f32x2 __attribute__((ext_vector_type(2)));
typedef unsigned u32x4 __attribute__((ext_vector_type(4)));
typedef unsigned u32x2 __attribute__((ext_vector_type(2)));
typedef unsigned short u16;

#define FUSED 1
#define WARM 0

constexpr unsigned WS_E = 0;
constexpr unsigned FS = 128;
constexpr unsigned WS_FX = 4096;
constexpr unsigned WS_FY = WS_FX + 256 * FS;
constexpr unsigned WS_FO = WS_FY + 256 * FS;
constexpr unsigned WS_L2 = WS_FO + 256 * FS;
constexpr unsigned WS_L1Z = WS_L2 + 8 * 32 * 272;
constexpr unsigned WS_L1K = WS_L1Z + 8 * 32 * 512;
constexpr unsigned WS_XT = WS_L1K + 8 * 32 * 32 * 256;
constexpr unsigned WS_Y = WS_XT + 512 * 4096 + 4096;
constexpr unsigned WS_O = WS_Y + 32 * 512 * 128;
constexpr unsigned WS_ST = WS_O + 128 * 16 * 1024;
constexpr unsigned WS_END = WS_ST + 512 * 4 * 16 * 8;

constexpr unsigned SLOT = 24576;
constexpr unsigned A_XCH = SLOT + 16384;
constexpr unsigned A_WPHI = 3 * SLOT;
constexpr unsigned A_GW = A_WPHI + 2048;
constexpr unsigned A_XZ = A_GW + 1024;
constexpr unsigned A_GP = A_XZ + 512;
constexpr unsigned A_DUMMY = A_GP + 1024;
constexpr unsigned A_WO = 0, A_Y = 65536;
constexpr unsigned TROW = 528, TSZ = 32 * TROW;
constexpr unsigned S_KR = 4 * TSZ, KSZ = 4608;
constexpr unsigned S_XT = S_KR;
constexpr unsigned UCP = 4160, USZ = 4 * UCP;
constexpr unsigned S_RED = 2 * USZ;
constexpr unsigned LDS_BYTES = 79872;
constexpr int SPIN_CAP = 1 << 17;

struct Params {
  const u16 *x, *wqkv, *wphi, *wo, *are, *aim, *bre, *bim, *cre, *cim, *logdt, *dskip, *gw;
  u16* out;
  char* ws;
};

DI unsigned pk2(float a, float b) { bf16x2 v; v[0] = (__bf16)a; v[1] = (__bf16)b; return __builtin_bit_cast(unsigned, v); }
DI float bf2f(u16 h) { return __uint_as_float((unsigned)h << 16); }
DI bf16x8 as_frag(u32x4 v) { return __builtin_bit_cast(bf16x8, v); }
DI bf16x8 pack8(const f32x16& a, int s) {
  u32x4 p;
  p[0] = pk2(a[8 * s + 0], a[8 * s + 1]); p[1] = pk2(a[8 * s + 2], a[8 * s + 3]);
  p[2] = pk2(a[8 * s + 4], a[8 * s + 5]); p[3] = pk2(a[8 * s + 6], a[8 * s + 7]);
  return as_frag(p);
}
DI void dma16(const void* sbase, unsigned voff, unsigned lds) {
  asm volatile("s_mov_b32 m0, %2\n\ts_nop 0\n\tglobal_load_lds_dwordx4 %0, %1" ::"v"(voff), "s"(sbase), "s"(lds) : "memory");
}
DI void dma16_sc1(const void* sbase, unsigned voff, unsigned lds) {
  asm volatile("s_mov_b32 m0, %2\n\ts_nop 0\n\tglobal_load_lds_dwordx4 %0, %1 sc1" ::"v"(voff), "s"(sbase), "s"(lds) : "memory");
}
DI void dma4(const void* sbase, unsigned voff, unsigned lds) {
  asm volatile("s_mov_b32 m0, %2\n\ts_nop 0\n\tglobal_load_lds_dword %0, %1" ::"v"(voff), "s"(sbase), "s"(lds) : "memory");
}
template <int N> DI void wait_vm_bar() { asm volatile("s_waitcnt vmcnt(%0) lgkmcnt(0)\n\ts_barrier" ::"n"(N) : "memory"); }
DI void bar_lgkm() { asm volatile("s_waitcnt lgkmcnt(0)\n\ts_barrier" ::: "memory"); }
DI bf16x8 lds_frag(unsigned a) { return *(LAS bf16x8*)a; }
DI bf16x4 lds_tr(unsigned a) { return __builtin_amdgcn_ds_read_tr16_b64_v4bf16((LAS bf16x4*)a); }
DI bf16x8 lds_tr2(unsigned a0, unsigned a1) { return __builtin_shufflevector(lds_tr(a0), lds_tr(a1), 0, 1, 2, 3, 4, 5, 6, 7); }
#define MFMA32(a, b, c) __builtin_amdgcn_mfma_f32_32x32x16_bf16((a), (b), (c), 0, 0, 0)
#define MFMA16(a, b, c) __builtin_amdgcn_mfma_f32_16x16x32_bf16((a), (b), (c), 0, 0, 0)
DI float fsum32(float a) {
  auto r = __builtin_amdgcn_permlane32_swap(__float_as_uint(a), __float_as_uint(a), false, false);
  unsigned r0 = r[0], r1 = r[1];
  return __uint_as_float(r0) + __uint_as_float(r1);
}
DI float fmax32(float a) {
  auto r = __builtin_amdgcn_permlane32_swap(__float_as_uint(a), __float_as_uint(a), false, false);
  unsigned r0 = r[0], r1 = r[1];
  return fmaxf(__uint_as_float(r0), __uint_as_float(r1));
}
DI float fmin32(float a) {
  auto r = __builtin_amdgcn_permlane32_swap(__float_as_uint(a), __float_as_uint(a), false, false);
  unsigned r0 = r[0], r1 = r[1];
  return fminf(__uint_as_float(r0), __uint_as_float(r1));
}
DI float fsum16(float a) {
  auto r = __builtin_amdgcn_permlane16_swap(__float_as_uint(a), __float_as_uint(a), false, false);
  unsigned r0 = r[0], r1 = r[1];
  return __uint_as_float(r0) + __uint_as_float(r1);
}
DI unsigned load_tag(const char* ws) {
  unsigned e;
  asm volatile("s_load_dword %0, %1, 0x0\n\ts_waitcnt lgkmcnt(0)" : "=s"(e) : "s"(ws) : "memory");
  return e;
}
DI unsigned mk_tag(unsigned e) { return (e + 1u) * 0x9E3779B1u + 0x7FEB352Du; }
typedef __amdgpu_buffer_rsrc_t rsrc_t;
DI u32x4 ld16_sc1(rsrc_t r, unsigned off) { return __builtin_amdgcn_raw_buffer_load_b128(r, off, 0, 16); }
DI u32x2 ld8_sc1(rsrc_t r, unsigned off) { return __builtin_amdgcn_raw_buffer_load_b64(r, off, 0, 16); }
DI unsigned ld4_sc1(rsrc_t r, unsigned off) { return __builtin_amdgcn_raw_buffer_load_b32(r, off, 0, 16); }
DI void st16_sc1(rsrc_t r, unsigned off, u32x4 v) { __builtin_amdgcn_raw_buffer_store_b128(v, r, off, 0, 16); }
DI void st8_sc1(rsrc_t r, unsigned off, u32x2 v) { __builtin_amdgcn_raw_buffer_store_b64(v, r, off, 0, 16); }
DI void st4_sc1(rsrc_t r, unsigned off, unsigned v) { __builtin_amdgcn_raw_buffer_store_b32(v, r, off, 0, 16); }
DI void wait_words(rsrc_t r, unsigned off, unsigned tag) {
#pragma unroll 1
  for (int sp = 0; sp < SPIN_CAP; ++sp) {
    asm volatile("" ::: "memory");
    unsigned f = ld4_sc1(r, off);
    if (__all(f == tag)) break;
    __builtin_amdgcn_s_sleep(2);
  }
  asm volatile("" ::: "memory");
}

#define STAMP(k)
#define STAMP_DECL
#define STAMP_FLUSH()

DI void dual_softmax(const f32x16& P, int e, bf16x8& fp, bf16x8& fm) {
  const float L = 1.44269504088896341f;
  float v[8];
#pragma unroll
  for (int j = 0; j < 8; ++j) v[j] = P[8 * e + j] * L;
  float mx = fmaxf(fmaxf(fmaxf(v[0], v[1]), fmaxf(v[2], v[3])), fmaxf(fmaxf(v[4], v[5]), fmaxf(v[6], v[7])));
  float mn = fminf(fminf(fminf(v[0], v[1]), fminf(v[2], v[3])), fminf(fminf(v[4], v[5]), fminf(v[6], v[7])));
  mx = fmax32(mx); mn = fmin32(mn);
  float a[8], b[8];
#pragma unroll
  for (int j = 0; j < 8; ++j) { a[j] = __builtin_amdgcn_exp2f(v[j] - mx); b[j] = __builtin_amdgcn_exp2f(mn - v[j]); }
  float sa = ((a[0] + a[1]) + (a[2] + a[3])) + ((a[4] + a[5]) + (a[6] + a[7]));
  float sb = ((b[0] + b[1]) + (b[2] + b[3])) + ((b[4] + b[5]) + (b[6] + b[7]));
  float ra = __builtin_amdgcn_rcpf(fsum32(sa)), rb = __builtin_amdgcn_rcpf(fsum32(sb));
  u32x4 x, y;
#pragma unroll
  for (int j = 0; j < 4; ++j) { x[j] = pk2(a[2 * j] * ra, a[2 * j + 1] * ra); y[j] = pk2(b[2 * j] * rb, b[2 * j + 1] * rb); }
  fp = as_frag(x); fm = as_frag(y);
}

DI void final_tile(const Params& p, rsrc_t ws, int ti, int q, int w, int lane, float gate, bool sc1) {
  const int i16 = lane & 15, g = lane >> 4, qq = i16 >> 2, pp = lane & 3;
  {
    const char* ysrc = p.ws + WS_Y + (unsigned)(ti * 512 + 64 * q + 16 * w) * 128u;
    const int rr = lane >> 3;
    const unsigned vo = rr * 128 + (((lane & 7) ^ (((rr >> 1) & 3) << 1)) << 4);
    dma16_sc1(ysrc, vo, A_Y + w * 2048);
    dma16_sc1(ysrc + 1024, vo, A_Y + w * 2048 + 1024);
  }
  u32x4 of[16];
  const unsigned obase = WS_O + (unsigned)(4 * ti + w) * 16384u + lane * 16;
#pragma unroll
  for (int k = 0; k < 16; ++k) of[k] = ld16_sc1(ws, obase + k * 1024);
  const int key = (2 * (g & 1) + (qq >> 1)) & 3;
  unsigned abase[4];
#pragma unroll
  for (int ct = 0; ct < 4; ++ct)
    abase[ct] = A_WO + (16 * (g >> 1) + 4 * (g & 1) + qq) * 128 + (((2 * ct + (pp >> 1)) ^ (key << 1)) << 4) + 8 * (pp & 1);
  f32x4 acc[4];
#pragma unroll
  for (int ct = 0; ct < 4; ++ct) acc[ct] = f32x4{0.f, 0.f, 0.f, 0.f};
#pragma unroll
  for (int k = 0; k < 16; ++k) {
#pragma unroll
    for (int ct = 0; ct < 4; ++ct) {
      bf16x8 a = lds_tr2(abase[ct] + k * 4096, abase[ct] + k * 4096 + 1024);
      acc[ct] = MFMA16(a, as_frag(of[k]), acc[ct]);
    }
  }
  wait_vm_bar<0>();
  const int ykey = (2 * g + (qq >> 1)) & 3;
  const unsigned ybase = A_Y + (4 * g + qq) * 128 + (((2 * w + (pp >> 1)) ^ (ykey << 1)) << 4) + 8 * (pp & 1);
  unsigned o2[4][2];
#pragma unroll
  for (int ct = 0; ct < 4; ++ct) {
    bf16x4 yb = lds_tr(ybase + ct * 2048);
    float r[4];
#pragma unroll
    for (int j = 0; j < 4; ++j) { float b = (float)yb[j]; r[j] = b + gate * (acc[ct][j] - b); }
    o2[ct][0] = pk2(r[0], r[1]); o2[ct][1] = pk2(r[2], r[3]);
  }
  char* orow = (char*)p.out + (unsigned)(64 * ti + 16 * w + i16) * 1024u + 128 * q + 32 * (g & 1) + 16 * (g >> 1);
#pragma unroll
  for (int c2 = 0; c2 < 2; ++c2) {
    auto s0 = __builtin_amdgcn_permlane16_swap(o2[2 * c2][0], o2[2 * c2 + 1][0], false, false);
    auto s1 = __builtin_amdgcn_permlane16_swap(o2[2 * c2][1], o2[2 * c2 + 1][1], false, false);
    u32x4 v; v[0] = s0[0]; v[1] = s1[0]; v[2] = s0[1]; v[3] = s1[1];
    *(u32x4*)(orow + 64 * c2) = v;
  }
}

DI void wo_dma(const Params& p, int q, int part, int nparts, int lane) {
  const int rr = lane >> 3;
  const unsigned vo = rr * 1024 + (((lane & 7) ^ (((rr >> 1) & 3) << 1)) << 4);
  const int n = 64 / nparts;
  const char* src = (const char*)p.wo + 128 * q + (unsigned)(part * n) * 8192u;
  unsigned dst = A_WO + part * n * 1024;
#pragma unroll 4
  for (int i = 0; i < n; ++i) dma16(src + i * 8192, vo, dst + i * 1024);
}

DI void att_block(const Params& p, int b) {
  STAMP_DECL;
  STAMP(0);
  const int tid = threadIdx.x, lane = tid & 63, w = __builtin_amdgcn_readfirstlane(tid >> 6);
  const int xcd = b & 7, bi = b >> 3, ti = 4 * xcd + (bi & 3), q = bi >> 2;
  const int th = w & 1, hp = w >> 1;
  const int r = lane & 31, h = lane >> 5, blk = (lane >> 4) & 1, i16 = lane & 15, qq = i16 >> 2, pp = lane & 3;
  __builtin_amdgcn_s_setprio(2);
  const int rr = lane >> 3, sl = lane & 7;
  const unsigned vx0 = rr * 1024 + ((sl ^ (rr >> 1)) << 4), vx1 = rr * 1024 + ((sl ^ (4 + (rr >> 1))) << 4);
  const unsigned vw = rr * 3072 + ((sl ^ (((rr >> 1) & 1) << 2)) << 4);
  const char* xs = (const char*)p.x + (unsigned)(64 * ti + 16 * w) * 1024u;
  const char* wsrc = (const char*)p.wqkv + (unsigned)(16 * w) * 3072u + 128 * q;
  const unsigned ldw = w * 2048;
  auto stage_kv = [&](int c, unsigned slot) {
    dma16(xs + c * 128, vx0, slot + ldw);
    dma16(xs + c * 128 + 8192, vx1, slot + ldw + 1024);
    dma16(wsrc + c * 196608 + 1024, vw, slot + 8192 + ldw);
    dma16(wsrc + c * 196608 + 1024 + 24576, vw, slot + 8192 + ldw + 1024);
    dma16(wsrc + c * 196608 + 2048, vw, slot + 16384 + ldw);
    dma16(wsrc + c * 196608 + 2048 + 24576, vw, slot + 16384 + ldw + 1024);
  };
  auto stage_q = [&](int c0, int n, unsigned slot) {
    for (int i = 0; i < n; ++i) {
      dma16(wsrc + (c0 + i) * 196608, vw, slot + i * 8192 + ldw);
      dma16(wsrc + (c0 + i) * 196608 + 24576, vw, slot + i * 8192 + ldw + 1024);
    }
  };
  if (w < 2) dma16((const char*)p.wphi + q * 2048 + w * 1024, lane * 16, A_WPHI + w * 1024);
  else if (w == 2) dma16((const char*)p.gw, lane * 16, A_GW);
  stage_kv(0, 0); stage_kv(1, SLOT); stage_kv(2, 2 * SLOT);
  constexpr int NW = 0;
  const unsigned tag = mk_tag(load_tag(p.ws + WS_E));
  const rsrc_t ws = __builtin_amdgcn_make_buffer_rsrc(p.ws, 0, WS_END, 0x00020000);

  unsigned xa[4];
#pragma unroll
  for (int s = 0; s < 4; ++s) xa[s] = (32 * th + r) * 128 + (((2 * s + h) ^ ((r >> 1) & 7)) << 4);
  const unsigned wa = (8 * h + qq) * 128 + (((4 * hp + 2 * blk + (pp >> 1)) ^ ((qq >> 1) << 2)) << 4) + 8 * (pp & 1);

  bf16x8 X[32];
  f32x16 ak, av;
#pragma unroll
  for (int i = 0; i < 16; ++i) { ak[i] = 0.f; av[i] = 0.f; }
#pragma unroll
  for (int c = 0; c < 8; ++c) {
    const unsigned slot = (c % 3) * SLOT;
    if (c < 3) wait_vm_bar<12 + NW>(); else wait_vm_bar<12>();
    if (c == 0) STAMP(1);
    bf16x8 wk[4], wv[4];
#pragma unroll
    for (int s = 0; s < 4; ++s) {
      X[4 * c + s] = lds_frag(slot + xa[s]);
      wk[s] = lds_tr2(slot + 8192 + wa + s * 2048, slot + 8192 + wa + s * 2048 + 512);
      wv[s] = lds_tr2(slot + 16384 + wa + s * 2048, slot + 16384 + wa + s * 2048 + 512);
    }
    bar_lgkm();
    if (c < 5) stage_kv(c + 3, slot);
    else if (c == 5) stage_q(0, 3, slot);
    else if (c == 6) stage_q(3, 3, slot);
    else stage_q(6, 2, slot);
#pragma unroll
    for (int s = 0; s < 4; ++s) {
      ak = MFMA32(wk[s], X[4 * c + s], ak);
      av = MFMA32(X[4 * c + s], wv[s], av);
    }
  }
  STAMP(2);
  bf16x8 A1, A2, SEL0, SEL1;
  {
    const unsigned a = A_WPHI + (2 * hp + blk) * 512 + (4 * h + qq) * 32 + 8 * pp;
    u32x4 wf = __builtin_bit_cast(u32x4, lds_tr2(a, a + 256));
    u32x4 z = {0u, 0u, 0u, 0u};
    A1 = as_frag(blk ? z : wf);
    A2 = as_frag(blk ? wf : z);
    u32x4 s0, s1;
#pragma unroll
    for (int d = 0; d < 4; ++d) {
      const int f0 = ((2 * d) & 3) + 8 * (d >> 1) + 4 * h;
      s0[d] = (r == f0 ? 0x3F80u : 0u) | (r == f0 + 1 ? 0x3F800000u : 0u);
      s1[d] = (r == f0 + 16 ? 0x3F80u : 0u) | (r == f0 + 17 ? 0x3F800000u : 0u);
    }
    SEL0 = as_frag(s0); SEL1 = as_frag(s1);
  }
  f32x16 zero16;
#pragma unroll
  for (int i = 0; i < 16; ++i) zero16[i] = 0.f;
  f32x16 R, Z;
  {
    f32x16 P = MFMA32(A1, pack8(ak, 0), zero16);
    P = MFMA32(A2, pack8(ak, 1), P);
    const u32x4 ones = {0x3F803F80u, 0x3F803F80u, 0x3F803F80u, 0x3F803F80u};
    const u32x4 v0 = __builtin_bit_cast(u32x4, pack8(av, 0)), v1 = __builtin_bit_cast(u32x4, pack8(av, 1));
    f32x16 Dh[2];
#pragma unroll
    for (int e = 0; e < 2; ++e) {
      bf16x8 fp, fm;
      dual_softmax(P, e, fp, fm);
      f32x16 T = MFMA32(fp, SEL0, zero16);
      T = MFMA32(fm, SEL1, T);
      const bool mine = (blk == e);
      Dh[e] = MFMA32(pack8(T, 0), as_frag(mine ? v0 : ones), zero16);
      Dh[e] = MFMA32(pack8(T, 1), as_frag(mine ? v1 : ones), Dh[e]);
    }
#pragma unroll
    for (int i = 0; i < 16; ++i) { R[i] = blk ? Dh[1][i] : Dh[0][i]; Z[i] = blk ? Dh[0][i] : Dh[1][i]; }
  }
  {
    float kr[8], kz[8];
    f32x4 s0, s1, z0, z1;
#pragma unroll
    for (int j = 0; j < 4; ++j) {
      s0[j] = th ? R[j] : R[8 + j]; s1[j] = th ? R[4 + j] : R[12 + j];
      z0[j] = th ? Z[j] : Z[8 + j]; z1[j] = th ? Z[4 + j] : Z[12 + j];
      kr[j] = th ? R[8 + j] : R[j]; kr[4 + j] = th ? R[12 + j] : R[4 + j];
      kz[j] = th ? Z[8 + j] : Z[j]; kz[4 + j] = th ? Z[12 + j] : Z[4 + j];
    }
    *(LAS f32x4*)(A_XCH + w * 2048 + lane * 16) = s0;
    *(LAS f32x4*)(A_XCH + w * 2048 + 1024 + lane * 16) = s1;
    if (i16 == 0) {
      *(LAS f32x4*)(A_XZ + w * 128 + (lane >> 4) * 32) = z0;
      *(LAS f32x4*)(A_XZ + w * 128 + (lane >> 4) * 32 + 16) = z1;
    }
    wait_vm_bar<0>();
    const f32x4 t0 = *(LAS f32x4*)(A_XCH + (w ^ 1) * 2048 + lane * 16), t1 = *(LAS f32x4*)(A_XCH + (w ^ 1) * 2048 + 1024 + lane * 16);
    const f32x4 y0 = *(LAS f32x4*)(A_XZ + (w ^ 1) * 128 + (lane >> 4) * 32), y1 = *(LAS f32x4*)(A_XZ + (w ^ 1) * 128 + (lane >> 4) * 32 + 16);
    const unsigned slk = (((2 * hp + blk) * 2 + th) * 2) * 2 + h;
    const unsigned slz = (((2 * hp + 1 - blk) * 2 + th) * 2) * 2 + h;
    const unsigned ok = WS_L1K + ((q * 32 + slk) * 32 + ti) * 256 + i16 * 16;
    st16_sc1(ws, ok, u32x4{pk2(kr[0] + t0[0], kr[1] + t0[1]), tag, pk2(kr[2] + t0[2], kr[3] + t0[3]), tag});
    st16_sc1(ws, ok + 2 * 32 * 256, u32x4{pk2(kr[4] + t1[0], kr[5] + t1[1]), tag, pk2(kr[6] + t1[2], kr[7] + t1[3]), tag});
    if (i16 == 0) {
      const unsigned oz = WS_L1Z + ((q * 32 + slz) * 32 + ti) * 16;
      st16_sc1(ws, oz, u32x4{pk2(kz[0] + y0[0], kz[1] + y0[1]), tag, pk2(kz[2] + y0[2], kz[3] + y0[3]), tag});
      st16_sc1(ws, oz + 2 * 32 * 16, u32x4{pk2(kz[4] + y1[0], kz[5] + y1[1]), tag, pk2(kz[6] + y1[2], kz[7] + y1[3]), tag});
    }
  }
  STAMP(3);
  bf16x8 pq[2][2];
  {
    f32x16 aq = zero16;
#pragma unroll
    for (int c = 0; c < 8; ++c) {
      const unsigned base = (c < 3 ? 2 * SLOT + c * 8192 : c < 6 ? (c - 3) * 8192 : SLOT + (c - 6) * 8192) + wa;
#pragma unroll
      for (int s = 0; s < 4; ++s) aq = MFMA32(lds_tr2(base + s * 2048, base + s * 2048 + 512), X[4 * c + s], aq);
    }
    bar_lgkm();
    STAMP(4);
    if (w >= 2) wo_dma(p, q, w - 2, 2, lane);
    f32x16 P = MFMA32(A1, pack8(aq, 0), zero16);
    P = MFMA32(A2, pack8(aq, 1), P);
    dual_softmax(P, 0, pq[0][0], pq[0][1]);
    dual_softmax(P, 1, pq[1][0], pq[1][1]);
  }
  STAMP(5);
  unsigned lo_bits = 0x00003F80u, hi_bits = 0x3F800000u;
  asm volatile("" : "+s"(lo_bits), "+s"(hi_bits));
  const bf16x2 LO = __builtin_bit_cast(bf16x2, lo_bits), HI = __builtin_bit_cast(bf16x2, hi_bits);
  if (w == 0) {
    const unsigned o = WS_L1K + ((q * 32 + ti) * 32 + (lane >> 4) * 8) * 256 + i16 * 16;
    u32x4 g[8];
#pragma unroll 1
  for (int sp = 0; sp < SPIN_CAP; ++sp) {
      asm volatile("" ::: "memory");
      bool ok = true;
#pragma unroll
      for (int i = 0; i < 8; ++i) g[i] = ld16_sc1(ws, o + i * 256);
#pragma unroll
      for (int i = 0; i < 8; ++i) ok &= (g[i][1] == tag) & (g[i][3] == tag);
      if (__all(ok)) break;
      __builtin_amdgcn_s_sleep(2);
    }
    float a = 0.f, bb = 0.f, c = 0.f, d = 0.f;
#pragma unroll
    for (int i = 0; i < 8; ++i) {
      const unsigned p0 = g[i][0], p1 = g[i][2];
      a = __builtin_amdgcn_fdot2_f32_bf16(__builtin_bit_cast(bf16x2, p0), LO, a, false);
      bb = __builtin_amdgcn_fdot2_f32_bf16(__builtin_bit_cast(bf16x2, p0), HI, bb, false);
      c = __builtin_amdgcn_fdot2_f32_bf16(__builtin_bit_cast(bf16x2, p1), LO, c, false);
      d = __builtin_amdgcn_fdot2_f32_bf16(__builtin_bit_cast(bf16x2, p1), HI, d, false);
    }
    auto ac = __builtin_amdgcn_permlane32_swap(__float_as_uint(a), __float_as_uint(c), false, false);
    auto bd = __builtin_amdgcn_permlane32_swap(__float_as_uint(bb), __float_as_uint(d), false, false);
    const unsigned ac0 = ac[0], ac1 = ac[1], bd0 = bd[0], bd1 = bd[1];
    const float s1 = fsum16(__uint_as_float(ac0) + __uint_as_float(ac1)), s2 = fsum16(__uint_as_float(bd0) + __uint_as_float(bd1));
    if (blk == 0) st8_sc1(ws, WS_L2 + (q * 32 + ti) * 272 + i16 * 16 + 8 * h, u32x2{pk2(s1, s2), tag});
  } else if (w == 1) {
    const unsigned o = WS_L1Z + ((q * 32 + ti) * 32 + r) * 16;
    u32x4 g;
#pragma unroll 1
  for (int sp = 0; sp < SPIN_CAP; ++sp) {
      asm volatile("" ::: "memory");
      g = ld16_sc1(ws, o);
      if (__all((g[1] == tag) & (g[3] == tag))) break;
      __builtin_amdgcn_s_sleep(2);
    }
    const unsigned pw = h ? g[2] : g[0];
    float a = __uint_as_float(pw << 16), bb = __uint_as_float(pw & 0xFFFF0000u);
#pragma unroll
    for (int m = 1; m < 16; m <<= 1) { a += __shfl_xor(a, m); bb += __shfl_xor(bb, m); }
    a = fsum16(a); bb = fsum16(bb);
    if (r == 0) st8_sc1(ws, WS_L2 + (q * 32 + ti) * 272 + 256 + 8 * h, u32x2{pk2(a, bb), tag});
  }
  STAMP(6);
  {
    float ga = 0.f, gb = 0.f;
    const unsigned gaddr = A_GW + hp * 512 + 16 * h;
#pragma unroll
    for (int s = 0; s < 16; ++s) {
      const u32x4 gv = *(LAS u32x4*)(gaddr + s * 32);
      const u32x4 xv = __builtin_bit_cast(u32x4, hp ? X[16 + s] : X[s]);
#pragma unroll
      for (int d = 0; d < 4; d += 2) {
        const unsigned x0 = xv[d], x1 = xv[d + 1], g0 = gv[d], g1 = gv[d + 1];
        ga = __builtin_amdgcn_fdot2_f32_bf16(__builtin_bit_cast(bf16x2, x0), __builtin_bit_cast(bf16x2, g0), ga, false);
        gb = __builtin_amdgcn_fdot2_f32_bf16(__builtin_bit_cast(bf16x2, x1), __builtin_bit_cast(bf16x2, g1), gb, false);
      }
    }
    *(LAS float*)(A_GP + (32 * th + r) * 16 + (2 * hp + h) * 4) = ga + gb;
  }
  STAMP(7);
  {
    const unsigned o = WS_L2 + (q * 32 + hp * 16 + h) * 272 + (r < 16 ? r : 16) * 16;
    u32x4 g[8];
#pragma unroll 1
  for (int sp = 0; sp < SPIN_CAP; ++sp) {
      asm volatile("" ::: "memory");
      bool ok = true;
#pragma unroll
      for (int i = 0; i < 8; ++i) g[i] = ld16_sc1(ws, o + i * 544);
#pragma unroll
      for (int i = 0; i < 8; ++i) ok &= (g[i][1] == tag) & (g[i][3] == tag);
      if (__all(ok)) break;
      __builtin_amdgcn_s_sleep(2);
    }
    STAMP(8);
    const unsigned oo = WS_O + ((4 * ti + 2 * th + blk) * 16 + 2 * q + hp) * 1024 + (i16 + 16 * h) * 16;
#pragma unroll
    for (int e = 0; e < 2; ++e) {
      const u32x4 f0 = {g[4 * e][0], g[4 * e][2], g[4 * e + 1][0], g[4 * e + 1][2]};
      const u32x4 f1 = {g[4 * e + 2][0], g[4 * e + 2][2], g[4 * e + 3][0], g[4 * e + 3][2]};
      f32x16 Dn = MFMA32(as_frag(f0), pq[e][0], zero16);
      Dn = MFMA32(as_frag(f1), pq[e][1], Dn);
      const float rd = __builtin_amdgcn_rcpf(Dn[8] + 1e-6f);
      const u32x4 ov = {pk2(Dn[0] * rd, Dn[1] * rd), pk2(Dn[2] * rd, Dn[3] * rd), pk2(Dn[4] * rd, Dn[5] * rd), pk2(Dn[6] * rd, Dn[7] * rd)};
      st16_sc1(ws, oo + e * 512, ov);
    }
  }
  wait_vm_bar<0>();
  if (tid == 0) st4_sc1(ws, WS_FO + (ti * 8 + q) * FS, tag);
  STAMP(9);
  const f32x4 gp = *(LAS f32x4*)(A_GP + (16 * w + i16) * 16);
  const float gate = __builtin_amdgcn_rcpf(1.f + __builtin_amdgcn_exp2f(-1.44269504088896341f * ((gp[0] + gp[1]) + (gp[2] + gp[3]))));
  wait_words(ws, lane < 32 ? WS_FY + (q * 32 + lane) * FS : WS_FO + (ti * 8 + (lane & 7)) * FS, tag);
  STAMP(10);
  final_tile(p, ws, ti, q, w, lane, gate, true);
  STAMP(11);
  STAMP_FLUSH();
}

DI f32x2 cmul(f32x2 a, f32x2 b, f32x2 bn) {
  f32x2 ax = {a[0], a[0]}, ay = {a[1], a[1]};
  return __builtin_elementwise_fma(ay, bn, ax * b);
}
DI f32x2 rot(f32x2 b) { return f32x2{-b[1], b[0]}; }
DI f32x2 cexp_l(float lr2, float li2, float l) {
  const float m = __builtin_amdgcn_exp2f(lr2 * l), ph = __builtin_amdgcn_fractf(li2 * l);
  return f32x2{m * __builtin_amdgcn_cosf(ph), m * __builtin_amdgcn_sinf(ph)};
}
DI void put(unsigned a, f32x2 v) { *(LAS unsigned*)a = pk2(v[0], v[1]); }

DI void ssm_block(const Params& p, int sidx) {
  STAMP_DECL;
  STAMP(0);
  const int tid = threadIdx.x, lane = tid & 63, w = __builtin_amdgcn_readfirstlane(tid >> 6);
  const int xcd = sidx & 7, bi = sidx >> 3;
  const int i16 = lane & 15, g = lane >> 4, qq = i16 >> 2, pp = lane & 3;
  const int chl = w >> 1, ch = 64 * xcd + 2 * bi + chl;
  {
    const char* xs = (const char*)p.x + (unsigned)(64 * bi + 16 * w) * 1024u + 128 * xcd;
    const unsigned vo = (lane >> 3) * 1024 + (lane & 7) * 16;
    dma16(xs, vo, S_XT + w * 2048);
    dma16(xs + 8192, vo, S_XT + w * 2048 + 1024);
  }
  const int n = tid & 127, pi = ch * 128 + n;
  const float are = bf2f(p.are[pi]), aim = bf2f(p.aim[pi]), bre = bf2f(p.bre[pi]), bim = bf2f(p.bim[pi]);
  const float cre = bf2f(p.cre[pi]), cim = bf2f(p.cim[pi]);
  const float ldt = bf2f(p.logdt[ch]), dsk = bf2f(p.dskip[ch]);
  const unsigned epoch = load_tag(p.ws + WS_E);
  const unsigned tag = mk_tag(epoch);
  const rsrc_t ws = __builtin_amdgcn_make_buffer_rsrc(p.ws, 0, WS_END, 0x00020000);
  wait_vm_bar<0>();
  STAMP(1);
  {
    const unsigned a = S_XT + (8 * g + qq) * 128 + (16 * w + 4 * pp) * 2;
    const unsigned o = WS_XT + (64 * xcd + 16 * w + i16) * 4096 + (64 * bi + 8 * g) * 2;
    st16_sc1(ws, o, __builtin_bit_cast(u32x4, lds_tr2(a, a + 512)));
    st16_sc1(ws, o + 64, __builtin_bit_cast(u32x4, lds_tr2(a + 4096, a + 4096 + 512)));
  }
  {
    const float dt = (float)(__bf16)expf(ldt);
    const float lr2 = dt * are * 1.44269504088896341f, li2 = dt * aim * 0.15915494309189535f;
    const f32x2 dB = {dt * bre, dt * bim};
    const f32x2 W = cmul(f32x2{cre, cim}, dB, rot(dB));
    const f32x2 E1 = cexp_l(lr2, li2, 1.f), E8 = cexp_l(lr2, li2, 8.f), G = cexp_l(lr2, li2, 256.f);
    const unsigned tx = chl * 2 * TSZ + n * 4, ty = tx + TSZ;
    const f32x2 E1n = rot(E1), Gn = rot(G);
    const f32x2 E2 = cmul(E1, E1, E1n), E2n = rot(E2);
    const f32x2 X1 = cmul(W, E1, E1n), X2 = cmul(W, E2, E2n), X3 = cmul(X1, E2, E2n), X4 = cmul(X2, E2, E2n);
    const f32x2 X5 = cmul(X3, E2, E2n), X6 = cmul(X4, E2, E2n), X7 = cmul(X5, E2, E2n), X8 = cmul(W, E8, rot(E8));
    put(tx + 0 * TROW, W); put(tx + 1 * TROW, X1); put(tx + 2 * TROW, X2); put(tx + 3 * TROW, X3);
    put(tx + 4 * TROW, X4); put(tx + 5 * TROW, X5); put(tx + 6 * TROW, X6); put(tx + 7 * TROW, X7);
    f32x2 A = cmul(W, G, Gn), B = cmul(X8, G, Gn), C = cmul(X4, G, Gn);
    put(tx + 8 * TROW, A); put(tx + 9 * TROW, cmul(X2, G, Gn)); put(tx + 10 * TROW, C);
    put(tx + 12 * TROW, C); put(tx + 13 * TROW, cmul(X6, G, Gn)); put(tx + 14 * TROW, B);
    A = cmul(A, G, Gn); B = cmul(B, G, Gn); C = cmul(C, G, Gn);
    put(tx + 16 * TROW, A); put(tx + 17 * TROW, C); put(tx + 20 * TROW, C); put(tx + 21 * TROW, B);
    A = cmul(A, G, Gn); B = cmul(B, G, Gn); C = cmul(C, G, Gn);
    put(tx + 18 * TROW, A); put(tx + 19 * TROW, C); put(tx + 22 * TROW, C); put(tx + 23 * TROW, B);
#pragma unroll
    for (int m = 0; m < 4; ++m) {
      A = cmul(A, G, Gn); B = cmul(B, G, Gn);
      put(tx + (24 + m) * TROW, A); put(tx + (28 + m) * TROW, B);
    }
    const f32x2 F1 = {E8[0], -E8[1]}, F1n = rot(F1);
    const f32x2 F2 = cmul(F1, F1, F1n), F2n = rot(F2);
    const f32x2 F4 = cmul(F2, F2, F2n), F4n = rot(F4);
    f32x2 y0 = {1.f, 0.f}, y1 = F1, y2 = F2, y3 = cmul(F2, F1, F1n);
    put(ty + 0 * TROW, y0); put(ty + 1 * TROW, y1); put(ty + 2 * TROW, y2); put(ty + 3 * TROW, y3);
    y0 = F4;
#pragma unroll
    for (int m = 1; m < 8; ++m) {
      if (m > 1) y0 = cmul(y0, F4, F4n);
      y1 = cmul(y1, F4, F4n); y2 = cmul(y2, F4, F4n); y3 = cmul(y3, F4, F4n);
      put(ty + (4 * m) * TROW, y0); put(ty + (4 * m + 1) * TROW, y1); put(ty + (4 * m + 2) * TROW, y2); put(ty + (4 * m + 3) * TROW, y3);
    }
  }
  wait_vm_bar<0>();
  if (tid == 0) st4_sc1(ws, WS_FX + (xcd * 32 + bi) * FS, tag);
  STAMP(2);
  if (w < 2) {
    const int r = lane & 31, h = lane >> 5;
    const unsigned ta = w * 2 * TSZ + r * TROW + 16 * h;
    f32x16 v;
#pragma unroll
    for (int i = 0; i < 16; ++i) v[i] = 0.f;
#pragma unroll
    for (int s = 0; s < 16; ++s) v = MFMA32(lds_frag(ta + 32 * s), lds_frag(ta + TSZ + 32 * s), v);
    const unsigned ka = S_KR + w * KSZ + (2044 - 8 * r - 4 * h - 1792) * 2;
    *(LAS u32x2*)(ka + 2 * 1792) = u32x2{pk2(v[3], v[2]), pk2(v[1], v[0])};
    *(LAS u32x2*)(ka + 2 * 1536) = u32x2{pk2(v[6], v[5]), pk2(v[4], v[4])};
    *(LAS u32x2*)(ka + 2 * 1280) = u32x2{pk2(v[9], h ? v[9] : v[8]), pk2(v[8], v[8])};
    *(LAS u32x2*)(ka + 2 * 1024) = u32x2{pk2(v[11], h ? v[11] : v[10]), pk2(v[10], v[10])};
#pragma unroll
    for (int a = 0; a < 4; ++a) {
      const float t = v[12 + a];
      auto sw = __builtin_amdgcn_permlane32_swap(__float_as_uint(t), __float_as_uint(t), false, false);
      const unsigned lo = sw[0];
      const float first = (r & 1) ? t : __uint_as_float(lo);
      *(LAS u32x2*)(ka + 2 * (768 - 256 * a)) = u32x2{pk2(t, t), pk2(t, first)};
    }
  } else if (w == 2) {
    if (lane < 60) *(LAS u32x4*)(S_KR + (lane >= 30 ? KSZ - 480 : 0) + 4096 + lane * 16) = u32x4{0u, 0u, 0u, 0u};
    wait_words(ws, WS_FX + (xcd * 32 + (lane & 31)) * FS, tag);
  }
  bar_lgkm();
  STAMP(3);
  {
    const unsigned o = WS_XT + ch * 4096 + n * 32;
    const u32x4 a = ld16_sc1(ws, o), b = ld16_sc1(ws, o + 16);
    const u32x2 c = ld8_sc1(ws, o + 32);
    const unsigned d[10] = {a[0], a[1], a[2], a[3], b[0], b[1], b[2], b[3], c[0], c[1]};
    unsigned e[9];
#pragma unroll
    for (int k = 0; k < 9; ++k) e[k] = __builtin_amdgcn_alignbit(d[k + 1], d[k], 16);
    const unsigned u = chl * USZ + 32 + n * 32;
    *(LAS u32x4*)(u) = a; *(LAS u32x4*)(u + 16) = b;
    *(LAS u32x4*)(u + UCP) = u32x4{e[0], e[1], e[2], e[3]}; *(LAS u32x4*)(u + UCP + 16) = u32x4{e[4], e[5], e[6], e[7]};
    *(LAS u32x4*)(u + 2 * UCP) = u32x4{d[1], d[2], d[3], d[4]}; *(LAS u32x4*)(u + 2 * UCP + 16) = u32x4{d[5], d[6], d[7], d[8]};
    *(LAS u32x4*)(u + 3 * UCP) = u32x4{e[1], e[2], e[3], e[4]}; *(LAS u32x4*)(u + 3 * UCP + 16) = u32x4{e[5], e[6], e[7], e[8]};
    if (n == 0) {
      const u32x4 z = {0u, 0u, 0u, 0u};
      const unsigned u0 = chl * USZ;
      *(LAS u32x4*)(u0) = z; *(LAS u32x4*)(u0 + 16) = z;
      *(LAS u32x4*)(u0 + UCP) = z; *(LAS u32x4*)(u0 + UCP + 16) = u32x4{0u, 0u, 0u, d[0] << 16};
      *(LAS u32x4*)(u0 + 2 * UCP) = z; *(LAS u32x4*)(u0 + 2 * UCP + 16) = u32x4{0u, 0u, 0u, d[0]};
      *(LAS u32x4*)(u0 + 3 * UCP) = z; *(LAS u32x4*)(u0 + 3 * UCP + 16) = u32x4{0u, 0u, d[0] << 16, e[0]};
    }
  }
  bar_lgkm();
  STAMP(4);
  f32x4 acc[8];
#pragma unroll
  for (int c = 0; c < 8; ++c) acc[c] = f32x4{0.f, 0.f, 0.f, 0.f};
  {
    const int sg = (i16 + 1) & 3;
    unsigned ua = chl * USZ + sg * UCP + (i16 + 1 - sg + 8 * g) * 2 + (w & 1) * 256;
    unsigned kb = S_KR + chl * KSZ + (240 - 16 * i16 + 8 * g) * 2 + (w & 1) * 256;
#pragma unroll 1
    for (int rho = 0; rho < 4; ++rho) {
      bf16x8 A[8], G[8];
#pragma unroll
      for (int m = 0; m < 8; ++m) {
        const u32x2 lo = *(LAS u32x2*)(ua + 512 * m), hi = *(LAS u32x2*)(ua + 512 * m + 8);
        A[m] = as_frag(u32x4{lo[0], lo[1], hi[0], hi[1]});
        G[m] = lds_frag(kb + 512 * (7 - m));
      }
#pragma unroll
      for (int c = 0; c < 8; ++c)
#pragma unroll
        for (int m = 0; m <= c; ++m) acc[c] = MFMA16(A[m], G[c - m], acc[c]);
      ua += 64; kb += 64;
    }
  }
  STAMP(5);
  {
    f32x4 keep[4];
    const unsigned ro = S_RED + w * 4096 + lane * 16, ri = S_RED + (w ^ 1) * 4096 + lane * 16;
    if (w & 1) {
#pragma unroll
      for (int k = 0; k < 4; ++k) { *(LAS f32x4*)(ro + k * 1024) = acc[k]; keep[k] = acc[4 + k]; }
    } else {
#pragma unroll
      for (int k = 0; k < 4; ++k) { *(LAS f32x4*)(ro + k * 1024) = acc[4 + k]; keep[k] = acc[k]; }
    }
    bar_lgkm();
    const int c0 = 4 * (w & 1);
    unsigned o2[4][2];
#pragma unroll
    for (int k = 0; k < 4; ++k) {
      const f32x4 t = *(LAS f32x4*)(ri + k * 1024);
      const u32x2 uu = *(LAS u32x2*)(chl * USZ + (256 * (c0 + k) + 16 * i16 + 4 * g + 16) * 2);
      const unsigned u0 = uu[0], u1 = uu[1];
      o2[k][0] = pk2(keep[k][0] + t[0] + dsk * __uint_as_float(u0 << 16), keep[k][1] + t[1] + dsk * __uint_as_float(u0 & 0xFFFF0000u));
      o2[k][1] = pk2(keep[k][2] + t[2] + dsk * __uint_as_float(u1 << 16), keep[k][3] + t[3] + dsk * __uint_as_float(u1 & 0xFFFF0000u));
    }
#pragma unroll
    for (int k = 0; k < 2; ++k) {
      auto s0 = __builtin_amdgcn_permlane16_swap(o2[2 * k][0], o2[2 * k + 1][0], false, false);
      auto s1 = __builtin_amdgcn_permlane16_swap(o2[2 * k][1], o2[2 * k + 1][1], false, false);
      u32x4 v; v[0] = s0[0]; v[1] = s1[0]; v[2] = s0[1]; v[3] = s1[1];
      const int t = 256 * (c0 + 2 * k + (g & 1)) + 16 * i16 + 8 * (g >> 1);
      st16_sc1(ws, WS_Y + ((t >> 6) * 512 + ch) * 128 + (t & 63) * 2, v);
    }
  }
  wait_vm_bar<0>();
  if (tid == 0) st4_sc1(ws, WS_FY + (xcd * 32 + bi) * FS, tag);
  STAMP(6);
  if (sidx == 0 && w == 0) {
#pragma unroll 1
    for (int k = 0; k < 4; ++k) wait_words(ws, WS_FX + (k * 64 + lane) * FS, tag);
    wait_words(ws, WS_L2 + (lane & 7) * 32 * 272 + 4, tag);
    if (lane == 0) st4_sc1(ws, WS_E, epoch + 1u);
  }
  STAMP(7);
  STAMP_FLUSH();
}

__global__ void __launch_bounds__(256, 2) hh_s4_mix_kernel(Params p) {
  if (blockIdx.x < 256) att_block(p, blockIdx.x);
  else ssm_block(p, blockIdx.x - 256);
}

extern "C" void kernel_launch(void* const* d_in, const int* in_sizes, int n_in, void* d_out, int out_size, void* d_ws,
                              size_t ws_size, hipStream_t stream) {
  Params p;
  p.x = (const u16*)d_in[0]; p.wqkv = (const u16*)d_in[1]; p.wphi = (const u16*)d_in[2]; p.wo = (const u16*)d_in[3];
  p.are = (const u16*)d_in[5]; p.aim = (const u16*)d_in[6]; p.bre = (const u16*)d_in[7]; p.bim = (const u16*)d_in[8];
  p.cre = (const u16*)d_in[9]; p.cim = (const u16*)d_in[10]; p.logdt = (const u16*)d_in[11]; p.dskip = (const u16*)d_in[12];
  p.gw = (const u16*)d_in[13];
  p.out = (u16*)d_out;
  p.ws = (char*)d_ws;
  (void)hipFuncSetAttribute((const void*)hh_s4_mix_kernel, hipFuncAttributeMaxDynamicSharedMemorySize, LDS_BYTES);
  hh_s4_mix_kernel<<<512, 256, LDS_BYTES, stream>>>(p);
}
```

```cpp
#include <hip/hip_runtime.h>
#include <hip/hip_bf16.h>
#include <stdint.h>
#pragma clang diagnostic ignored "-Wint-to-pointer-cast"

#define LAS __attribute__((address_space(3)))
#define GAS __attribute__((address_space(1)))
#define DI __device__ __forceinline__
typedef __bf16 bf16x8 __attribute__((ext_vector_type(8)));
typedef __bf16 bf16x4 __attribute__((ext_vector_type(4)));
typedef __bf16 bf16x2 __attribute__((ext_vector_type(2)));
typedef float f32x16 __attribute__((ext_vector_type(16)));
typedef float f32x4 __attribute__((ext_vector_type(4)));
typedef float f32x2 __attribute__((ext_vector_type(2)));
typedef unsigned u32x4 __attribute__((ext_vector_type(4)));
typedef unsigned u32x2 __attribute__((ext_vector_type(2)));
typedef unsigned short u16;

#define FUSED 1
#define WARM 0

constexpr unsigned WS_E = 0;
constexpr unsigned FS = 128;
constexpr unsigned WS_FX = 4096;
constexpr unsigned WS_FY = WS_FX + 256 * FS;
constexpr unsigned WS_FO = WS_FY + 256 * FS;
constexpr unsigned WS_L2 = WS_FO + 256 * FS;
constexpr unsigned WS_L1Z = WS_L2 + 8 * 32 * 272;
constexpr unsigned WS_L1K = WS_L1Z + 8 * 32 * 512;
constexpr unsigned WS_XT = WS_L1K + 8 * 32 * 32 * 256;
constexpr unsigned WS_Y = WS_XT + 512 * 4096 + 4096;
constexpr unsigned WS_O = WS_Y + 32 * 512 * 128;
constexpr unsigned WS_ST = WS_O + 128 * 16 * 1024;
constexpr unsigned WS_END = WS_ST + 512 * 4 * 16 * 8;

constexpr unsigned SLOT = 24576;
constexpr unsigned A_XCH = SLOT + 16384;
constexpr unsigned A_WPHI = 3 * SLOT;
constexpr unsigned A_GW = A_WPHI + 2048;
constexpr unsigned A_XZ = A_GW + 1024;
constexpr unsigned A_GP = A_XZ + 512;
constexpr unsigned A_DUMMY = A_GP + 1024;
constexpr unsigned A_WO = 0, A_Y = 65536;
constexpr unsigned TROW = 528, TSZ = 32 * TROW;
constexpr unsigned S_KR = 4 * TSZ, KSZ = 4608;
constexpr unsigned S_XT = S_KR;
constexpr unsigned UCP = 4160, USZ = 4 * UCP;
constexpr unsigned S_RED = 2 * USZ;
constexpr unsigned LDS_BYTES = 79872;
constexpr int SPIN_CAP = 1 << 17;

struct Params {
  const u16 *x, *wqkv, *wphi, *wo, *are, *aim, *bre, *bim, *cre, *cim, *logdt, *dskip, *gw;
  u16* out;
  char* ws;
};

DI unsigned pk2(float a, float b) { bf16x2 v; v[0] = (__bf16)a; v[1] = (__bf16)b; return __builtin_bit_cast(unsigned, v); }
DI float bf2f(u16 h) { return __uint_as_float((unsigned)h << 16); }
DI bf16x8 as_frag(u32x4 v) { return __builtin_bit_cast(bf16x8, v); }
DI bf16x8 pack8(const f32x16& a, int s) {
  u32x4 p;
  p[0] = pk2(a[8 * s + 0], a[8 * s + 1]); p[1] = pk2(a[8 * s + 2], a[8 * s + 3]);
  p[2] = pk2(a[8 * s + 4], a[8 * s + 5]); p[3] = pk2(a[8 * s + 6], a[8 * s + 7]);
  return as_frag(p);
}
DI void dma16(const void* sbase, unsigned voff, unsigned lds) {
  asm volatile("s_mov_b32 m0, %2\n\ts_nop 0\n\tglobal_load_lds_dwordx4 %0, %1" ::"v"(voff), "s"(sbase), "s"(lds) : "memory");
}
DI void dma16_sc1(const void* sbase, unsigned voff, unsigned lds) {
  asm volatile("s_mov_b32 m0, %2\n\ts_nop 0\n\tglobal_load_lds_dwordx4 %0, %1 sc1" ::"v"(voff), "s"(sbase), "s"(lds) : "memory");
}
DI void dma4(const void* sbase, unsigned voff, unsigned lds) {
  asm volatile("s_mov_b32 m0, %2\n\ts_nop 0\n\tglobal_load_lds_dword %0, %1" ::"v"(voff), "s"(sbase), "s"(lds) : "memory");
}
template <int N> DI void wait_vm_bar() { asm volatile("s_waitcnt vmcnt(%0) lgkmcnt(0)\n\ts_barrier" ::"n"(N) : "memory"); }
DI void bar_lgkm() { asm volatile("s_waitcnt lgkmcnt(0)\n\ts_barrier" ::: "memory"); }
DI bf16x8 lds_frag(unsigned a) { return *(LAS bf16x8*)a; }
DI bf16x4 lds_tr(unsigned a) { return __builtin_amdgcn_ds_read_tr16_b64_v4bf16((LAS bf16x4*)a); }
DI bf16x8 lds_tr2(unsigned a0, unsigned a1) { return __builtin_shufflevector(lds_tr(a0), lds_tr(a1), 0, 1, 2, 3, 4, 5, 6, 7); }
#define MFMA32(a, b, c) __builtin_amdgcn_mfma_f32_32x32x16_bf16((a), (b), (c), 0, 0, 0)
#define MFMA16(a, b, c) __builtin_amdgcn_mfma_f32_16x16x32_bf16((a), (b), (c), 0, 0, 0)
DI float fsum32(float a) {
  auto r = __builtin_amdgcn_permlane32_swap(__float_as_uint(a), __float_as_uint(a), false, false);
  unsigned r0 = r[0], r1 = r[1];
  return __uint_as_float(r0) + __uint_as_float(r1);
}
DI float fmax32(float a) {
  auto r = __builtin_amdgcn_permlane32_swap(__float_as_uint(a), __float_as_uint(a), false, false);
  unsigned r0 = r[0], r1 = r[1];
  return fmaxf(__uint_as_float(r0), __uint_as_float(r1));
}
DI float fmin32(float a) {
  auto r = __builtin_amdgcn_permlane32_swap(__float_as_uint(a), __float_as_uint(a), false, false);
  unsigned r0 = r[0], r1 = r[1];
  return fminf(__uint_as_float(r0), __uint_as_float(r1));
}
DI float fsum16(float a) {
  auto r = __builtin_amdgcn_permlane16_swap(__float_as_uint(a), __float_as_uint(a), false, false);
  unsigned r0 = r[0], r1 = r[1];
  return __uint_as_float(r0) + __uint_as_float(r1);
}
DI unsigned load_tag(const char* ws) {
  unsigned e;
  asm volatile("s_load_dword %0, %1, 0x0\n\ts_waitcnt lgkmcnt(0)" : "=s"(e) : "s"(ws) : "memory");
  return e;
}
DI unsigned mk_tag(unsigned e) { return (e + 1u) * 0x9E3779B1u + 0x7FEB352Du; }
typedef __amdgpu_buffer_rsrc_t rsrc_t;
DI u32x4 ld16_sc1(rsrc_t r, unsigned off) { return __builtin_amdgcn_raw_buffer_load_b128(r, off, 0, 16); }
DI u32x2 ld8_sc1(rsrc_t r, unsigned off) { return __builtin_amdgcn_raw_buffer_load_b64(r, off, 0, 16); }
DI unsigned ld4_sc1(rsrc_t r, unsigned off) { return __builtin_amdgcn_raw_buffer_load_b32(r, off, 0, 16); }
DI void st16_sc1(rsrc_t r, unsigned off, u32x4 v) { __builtin_amdgcn_raw_buffer_store_b128(v, r, off, 0, 16); }
DI void st8_sc1(rsrc_t r, unsigned off, u32x2 v) { __builtin_amdgcn_raw_buffer_store_b64(v, r, off, 0, 16); }
DI void st4_sc1(rsrc_t r, unsigned off, unsigned v) { __builtin_amdgcn_raw_buffer_store_b32(v, r, off, 0, 16); }
DI void wait_words(rsrc_t r, unsigned off, unsigned tag) {
#pragma unroll 1
  for (int sp = 0; sp < SPIN_CAP; ++sp) {
    asm volatile("" ::: "memory");
    unsigned f = ld4_sc1(r, off);
    if (__all(f == tag)) break;
    __builtin_amdgcn_s_sleep(2);
  }
  asm volatile("" ::: "memory");
}

#define STAMP(k)
#define STAMP_DECL
#define STAMP_FLUSH()

DI void dual_softmax(const f32x16& P, int e, bf16x8& fp, bf16x8& fm) {
  const float L = 1.44269504088896341f;
  float v[8];
#pragma unroll
  for (int j = 0; j < 8; ++j) v[j] = P[8 * e + j] * L;
  float mx = fmaxf(fmaxf(fmaxf(v[0], v[1]), fmaxf(v[2], v[3])), fmaxf(fmaxf(v[4], v[5]), fmaxf(v[6], v[7])));
  float mn = fminf(fminf(fminf(v[0], v[1]), fminf(v[2], v[3])), fminf(fminf(v[4], v[5]), fminf(v[6], v[7])));
  mx = fmax32(mx); mn = fmin32(mn);
  float a[8], b[8];
#pragma unroll
  for (int j = 0; j < 8; ++j) { a[j] = __builtin_amdgcn_exp2f(v[j] - mx); b[j] = __builtin_amdgcn_exp2f(mn - v[j]); }
  float sa = ((a[0] + a[1]) + (a[2] + a[3])) + ((a[4] + a[5]) + (a[6] + a[7]));
  float sb = ((b[0] + b[1]) + (b[2] + b[3])) + ((b[4] + b[5]) + (b[6] + b[7]));
  float ra = __builtin_amdgcn_rcpf(fsum32(sa)), rb = __builtin_amdgcn_rcpf(fsum32(sb));
  u32x4 x, y;
#pragma unroll
  for (int j = 0; j < 4; ++j) { x[j] = pk2(a[2 * j] * ra, a[2 * j + 1] * ra); y[j] = pk2(b[2 * j] * rb, b[2 * j + 1] * rb); }
  fp = as_frag(x); fm = as_frag(y);
}

DI void final_tile(const Params& p, rsrc_t ws, int ti, int q, int w, int lane, float gate, bool sc1) {
  const int i16 = lane & 15, g = lane >> 4, qq = i16 >> 2, pp = lane & 3;
  {
    const char* ysrc = p.ws + WS_Y + (unsigned)(ti * 512 + 64 * q + 16 * w) * 128u;
    const int rr = lane >> 3;
    const unsigned vo = rr * 128 + (((lane & 7) ^ (((rr >> 1) & 3) << 1)) << 4);
    dma16_sc1(ysrc, vo, A_Y + w * 2048);
    dma16_sc1(ysrc + 1024, vo, A_Y + w * 2048 + 1024);
  }
  u32x4 of[16];
  const unsigned obase = WS_O + (unsigned)(4 * ti + w) * 16384u + lane * 16;
#pragma unroll
  for (int k = 0; k < 16; ++k) of[k] = ld16_sc1(ws, obase + k * 1024);
  const int key = (2 * (g & 1) + (qq >> 1)) & 3;
  unsigned abase[4];
#pragma unroll
  for (int ct = 0; ct < 4; ++ct)
    abase[ct] = A_WO + (16 * (g >> 1) + 4 * (g & 1) + qq) * 128 + (((2 * ct + (pp >> 1)) ^ (key << 1)) << 4) + 8 * (pp & 1);
  f32x4 acc[4];
#pragma unroll
  for (int ct = 0; ct < 4; ++ct) acc[ct] = f32x4{0.f, 0.f, 0.f, 0.f};
#pragma unroll
  for (int k = 0; k < 16; ++k) {
#pragma unroll
    for (int ct = 0; ct < 4; ++ct) {
      bf16x8 a = lds_tr2(abase[ct] + k * 4096, abase[ct] + k * 4096 + 1024);
      acc[ct] = MFMA16(a, as_frag(of[k]), acc[ct]);
    }
  }
  wait_vm_bar<0>();
  const int ykey = (2 * g + (qq >> 1)) & 3;
  const unsigned ybase = A_Y + (4 * g + qq) * 128 + (((2 * w + (pp >> 1)) ^ (ykey << 1)) << 4) + 8 * (pp & 1);
  unsigned o2[4][2];
#pragma unroll
  for (int ct = 0; ct < 4; ++ct) {
    bf16x4 yb = lds_tr(ybase + ct * 2048);
    float r[4];
#pragma unroll
    for (int j = 0; j < 4; ++j) { float b = (float)yb[j]; r[j] = b + gate * (acc[ct][j] - b); }
    o2[ct][0] = pk2(r[0], r[1]); o2[ct][1] = pk2(r[2], r[3]);
  }
  char* orow = (char*)p.out + (unsigned)(64 * ti + 16 * w + i16) * 1024u + 128 * q + 32 * (g & 1) + 16 * (g >> 1);
#pragma unroll
  for (int c2 = 0; c2 < 2; ++c2) {
    auto s0 = __builtin_amdgcn_permlane16_swap(o2[2 * c2][0], o2[2 * c2 + 1][0], false, false);
    auto s1 = __builtin_amdgcn_permlane16_swap(o2[2 * c2][1], o2[2 * c2 + 1][1], false, false);
    u32x4 v; v[0] = s0[0]; v[1] = s1[0]; v[2] = s0[1]; v[3] = s1[1];
    *(u32x4*)(orow + 64 * c2) = v;
  }
}

DI void wo_dma(const Params& p, int q, int part, int nparts, int lane) {
  const int rr = lane >> 3;
  const unsigned vo = rr * 1024 + (((lane & 7) ^ (((rr >> 1) & 3) << 1)) << 4);
  const int n = 64 / nparts;
  const char* src = (const char*)p.wo + 128 * q + (unsigned)(part * n) * 8192u;
  unsigned dst = A_WO + part * n * 1024;
#pragma unroll 4
  for (int i = 0; i < n; ++i) dma16(src + i * 8192, vo, dst + i * 1024);
}

DI void att_block(const Params& p, int b) {
  STAMP_DECL;
  STAMP(0);
  const int tid = threadIdx.x, lane = tid & 63, w = __builtin_amdgcn_readfirstlane(tid >> 6);
  const int xcd = b & 7, bi = b >> 3, ti = 4 * xcd + (bi & 3), q = bi >> 2;
  const int th = w & 1, hp = w >> 1;
  const int r = lane & 31, h = lane >> 5, blk = (lane >> 4) & 1, i16 = lane & 15, qq = i16 >> 2, pp = lane & 3;
  __builtin_amdgcn_s_setprio(2);
  const int rr = lane >> 3, sl = lane & 7;
  const unsigned vx0 = rr * 1024 + ((sl ^ (rr >> 1)) << 4), vx1 = rr * 1024 + ((sl ^ (4 + (rr >> 1))) << 4);
  const unsigned vw = rr * 3072 + ((sl ^ (((rr >> 1) & 1) << 2)) << 4);
  const char* xs = (const char*)p.x + (unsigned)(64 * ti + 16 * w) * 1024u;
  const char* wsrc = (const char*)p.wqkv + (unsigned)(16 * w) * 3072u + 128 * q;
  const unsigned ldw = w * 2048;
  auto stage_kv = [&](int c, unsigned slot) {
    dma16(xs + c * 128, vx0, slot + ldw);
    dma16(xs + c * 128 + 8192, vx1, slot + ldw + 1024);
    dma16(wsrc + c * 196608 + 1024, vw, slot + 8192 + ldw);
    dma16(wsrc + c * 196608 + 1024 + 24576, vw, slot + 8192 + ldw + 1024);
    dma16(wsrc + c * 196608 + 2048, vw, slot + 16384 + ldw);
    dma16(wsrc + c * 196608 + 2048 + 24576, vw, slot + 16384 + ldw + 1024);
  };
  auto stage_q = [&](int c0, int n, unsigned slot) {
    for (int i = 0; i < n; ++i) {
      dma16(wsrc + (c0 + i) * 196608, vw, slot + i * 8192 + ldw);
      dma16(wsrc + (c0 + i) * 196608 + 24576, vw, slot + i * 8192 + ldw + 1024);
    }
  };
  if (w < 2) dma16((const char*)p.wphi + q * 2048 + w * 1024, lane * 16, A_WPHI + w * 1024);
  else if (w == 2) dma16((const char*)p.gw, lane * 16, A_GW);
  stage_kv(0, 0); stage_kv(1, SLOT); stage_kv(2, 2 * SLOT);
  constexpr int NW = 0;
  const unsigned tag = mk_tag(load_tag(p.ws + WS_E));
  const rsrc_t ws = __builtin_amdgcn_make_buffer_rsrc(p.ws, 0, WS_END, 0x00020000);

  unsigned xa[4];
#pragma unroll
  for (int s = 0; s < 4; ++s) xa[s] = (32 * th + r) * 128 + (((2 * s + h) ^ ((r >> 1) & 7)) << 4);
  const unsigned wa = (8 * h + qq) * 128 + (((4 * hp + 2 * blk + (pp >> 1)) ^ ((qq >> 1) << 2)) << 4) + 8 * (pp & 1);

  bf16x8 X[32];
  f32x16 ak, av;
#pragma unroll
  for (int i = 0; i < 16; ++i) { ak[i] = 0.f; av[i] = 0.f; }
#pragma unroll
  for (int c = 0; c < 8; ++c) {
    const unsigned slot = (c % 3) * SLOT;
    if (c < 3) wait_vm_bar<12 + NW>(); else wait_vm_bar<12>();
    if (c == 0) STAMP(1);
    bf16x8 wk[4], wv[4];
#pragma unroll
    for (int s = 0; s < 4; ++s) {
      X[4 * c + s] = lds_frag(slot + xa[s]);
      wk[s] = lds_tr2(slot + 8192 + wa + s * 2048, slot + 8192 + wa + s * 2048 + 512);
      wv[s] = lds_tr2(slot + 16384 + wa + s * 2048, slot + 16384 + wa + s * 2048 + 512);
    }
    bar_lgkm();
    if (c < 5) stage_kv(c + 3, slot);
    else if (c == 5) stage_q(0, 3, slot);
    else if (c == 6) stage_q(3, 3, slot);
    else stage_q(6, 2, slot);
#pragma unroll
    for (int s = 0; s < 4; ++s) {
      ak = MFMA32(wk[s], X[4 * c + s], ak);
      av = MFMA32(X[4 * c + s], wv[s], av);
    }
  }
  STAMP(2);
  bf16x8 A1, A2, SEL0, SEL1;
  {
    const unsigned a = A_WPHI + (2 * hp + blk) * 512 + (4 * h + qq) * 32 + 8 * pp;
    u32x4 wf = __builtin_bit_cast(u32x4, lds_tr2(a, a + 256));
    u32x4 z = {0u, 0u, 0u, 0u};
    A1 = as_frag(blk ? z : wf);
    A2 = as_frag(blk ? wf : z);
    u32x4 s0, s1;
#pragma unroll
    for (int d = 0; d < 4; ++d) {
      const int f0 = ((2 * d) & 3) + 8 * (d >> 1) + 4 * h;
      s0[d] = (r == f0 ? 0x3F80u : 0u) | (r == f0 + 1 ? 0x3F800000u : 0u);
      s1[d] = (r == f0 + 16 ? 0x3F80u : 0u) | (r == f0 + 17 ? 0x3F800000u : 0u);
    }
    SEL0 = as_frag(s0); SEL1 = as_frag(s1);
  }
  f32x16 zero16;
#pragma unroll
  for (int i = 0; i < 16; ++i) zero16[i] = 0.f;
  f32x16 R, Z;
  {
    f32x16 P = MFMA32(A1, pack8(ak, 0), zero16);
    P = MFMA32(A2, pack8(ak, 1), P);
    const u32x4 ones = {0x3F803F80u, 0x3F803F80u, 0x3F803F80u, 0x3F803F80u};
    const u32x4 v0 = __builtin_bit_cast(u32x4, pack8(av, 0)), v1 = __builtin_bit_cast(u32x4, pack8(av, 1));
    f32x16 Dh[2];
#pragma unroll
    for (int e = 0; e < 2; ++e) {
      bf16x8 fp, fm;
      dual_softmax(P, e, fp, fm);
      f32x16 T = MFMA32(fp, SEL0, zero16);
      T = MFMA32(fm, SEL1, T);
      const bool mine = (blk == e);
      Dh[e] = MFMA32(pack8(T, 0), as_frag(mine ? v0 : ones), zero16);
      Dh[e] = MFMA32(pack8(T, 1), as_frag(mine ? v1 : ones), Dh[e]);
    }
#pragma unroll
    for (int i = 0; i < 16; ++i) { R[i] = blk ? Dh[1][i] : Dh[0][i]; Z[i] = blk ? Dh[0][i] : Dh[1][i]; }
  }
  {
    float kr[8], kz[8];
    f32x4 s0, s1, z0, z1;
#pragma unroll
    for (int j = 0; j < 4; ++j) {
      s0[j] = th ? R[j] : R[8 + j]; s1[j] = th ? R[4 + j] : R[12 + j];
      z0[j] = th ? Z[j] : Z[8 + j]; z1[j] = th ? Z[4 + j] : Z[12 + j];
      kr[j] = th ? R[8 + j] : R[j]; kr[4 + j] = th ? R[12 + j] : R[4 + j];
      kz[j] = th ? Z[8 + j] : Z[j]; kz[4 + j] = th ? Z[12 + j] : Z[4 + j];
    }
    *(LAS f32x4*)(A_XCH + w * 2048 + lane * 16) = s0;
    *(LAS f32x4*)(A_XCH + w * 2048 + 1024 + lane * 16) = s1;
    if (i16 == 0) {
      *(LAS f32x4*)(A_XZ + w * 128 + (lane >> 4) * 32) = z0;
      *(LAS f32x4*)(A_XZ + w * 128 + (lane >> 4) * 32 + 16) = z1;
    }
    wait_vm_bar<0>();
    const f32x4 t0 = *(LAS f32x4*)(A_XCH + (w ^ 1) * 2048 + lane * 16), t1 = *(LAS f32x4*)(A_XCH + (w ^ 1) * 2048 + 1024 + lane * 16);
    const f32x4 y0 = *(LAS f32x4*)(A_XZ + (w ^ 1) * 128 + (lane >> 4) * 32), y1 = *(LAS f32x4*)(A_XZ + (w ^ 1) * 128 + (lane >> 4) * 32 + 16);
    const unsigned slk = (((2 * hp + blk) * 2 + th) * 2) * 2 + h;
    const unsigned slz = (((2 * hp + 1 - blk) * 2 + th) * 2) * 2 + h;
    const unsigned ok = WS_L1K + ((q * 32 + slk) * 32 + ti) * 256 + i16 * 16;
    st16_sc1(ws, ok, u32x4{pk2(kr[0] + t0[0], kr[1] + t0[1]), tag, pk2(kr[2] + t0[2], kr[3] + t0[3]), tag});
    st16_sc1(ws, ok + 2 * 32 * 256, u32x4{pk2(kr[4] + t1[0], kr[5] + t1[1]), tag, pk2(kr[6] + t1[2], kr[7] + t1[3]), tag});
    if (i16 == 0) {
      const unsigned oz = WS_L1Z + ((q * 32 + slz) * 32 + ti) * 16;
      st16_sc1(ws, oz, u32x4{pk2(kz[0] + y0[0], kz[1] + y0[1]), tag, pk2(kz[2] + y0[2], kz[3] + y0[3]), tag});
      st16_sc1(ws, oz + 2 * 32 * 16, u32x4{pk2(kz[4] + y1[0], kz[5] + y1[1]), tag, pk2(kz[6] + y1[2], kz[7] + y1[3]), tag});
    }
  }
  STAMP(3);
  bf16x8 pq[2][2];
  {
    f32x16 aq = zero16;
#pragma unroll
    for (int c = 0; c < 8; ++c) {
      const unsigned base = (c < 3 ? 2 * SLOT + c * 8192 : c < 6 ? (c - 3) * 8192 : SLOT + (c - 6) * 8192) + wa;
#pragma unroll
      for (int s = 0; s < 4; ++s) aq = MFMA32(lds_tr2(base + s * 2048, base + s * 2048 + 512), X[4 * c + s], aq);
    }
    bar_lgkm();
    STAMP(4);
    if (w >= 2) wo_dma(p, q, w - 2, 2, lane);
    f32x16 P = MFMA32(A1, pack8(aq, 0), zero16);
    P = MFMA32(A2, pack8(aq, 1), P);
    dual_softmax(P, 0, pq[0][0], pq[0][1]);
    dual_softmax(P, 1, pq[1][0], pq[1][1]);
  }
  STAMP(5);
  unsigned lo_bits = 0x00003F80u, hi_bits = 0x3F800000u;
  asm volatile("" : "+s"(lo_bits), "+s"(hi_bits));
  const bf16x2 LO = __builtin_bit_cast(bf16x2, lo_bits), HI = __builtin_bit_cast(bf16x2, hi_bits);
  if (w == 0) {
    const unsigned o = WS_L1K + ((q * 32 + ti) * 32 + (lane >> 4) * 8) * 256 + i16 * 16;
    u32x4 g[8];
#pragma unroll 1
  for (int sp = 0; sp < SPIN_CAP; ++sp) {
      asm volatile("" ::: "memory");
      bool ok = true;
#pragma unroll
      for (int i = 0; i < 8; ++i) g[i] = ld16_sc1(ws, o + i * 256);
#pragma unroll
      for (int i = 0; i < 8; ++i) ok &= (g[i][1] == tag) & (g[i][3] == tag);
      if (__all(ok)) break;
      __builtin_amdgcn_s_sleep(2);
    }
    float a = 0.f, bb = 0.f, c = 0.f, d = 0.f;
#pragma unroll
    for (int i = 0; i < 8; ++i) {
      const unsigned p0 = g[i][0], p1 = g[i][2];
      a = __builtin_amdgcn_fdot2_f32_bf16(__builtin_bit_cast(bf16x2, p0), LO, a, false);
      bb = __builtin_amdgcn_fdot2_f32_bf16(__builtin_bit_cast(bf16x2, p0), HI, bb, false);
      c = __builtin_amdgcn_fdot2_f32_bf16(__builtin_bit_cast(bf16x2, p1), LO, c, false);
      d = __builtin_amdgcn_fdot2_f32_bf16(__builtin_bit_cast(bf16x2, p1), HI, d, false);
    }
    auto ac = __builtin_amdgcn_permlane32_swap(__float_as_uint(a), __float_as_uint(c), false, false);
    auto bd = __builtin_amdgcn_permlane32_swap(__float_as_uint(bb), __float_as_uint(d), false, false);
    const unsigned ac0 = ac[0], ac1 = ac[1], bd0 = bd[0], bd1 = bd[1];
    const float s1 = fsum16(__uint_as_float(ac0) + __uint_as_float(ac1)), s2 = fsum16(__uint_as_float(bd0) + __uint_as_float(bd1));
    if (blk == 0) st8_sc1(ws, WS_L2 + (q * 32 + ti) * 272 + i16 * 16 + 8 * h, u32x2{pk2(s1, s2), tag});
  } else if (w == 1) {
    const unsigned o = WS_L1Z + ((q * 32 + ti) * 32 + r) * 16;
    u32x4 g;
#pragma unroll 1
  for (int sp = 0; sp < SPIN_CAP; ++sp) {
      asm volatile("" ::: "memory");
      g = ld16_sc1(ws, o);
      if (__all((g[1] == tag) & (g[3] == tag))) break;
      __builtin_amdgcn_s_sleep(2);
    }
    const unsigned pw = h ? g[2] : g[0];
    float a = __uint_as_float(pw << 16), bb = __uint_as_float(pw & 0xFFFF0000u);
#pragma unroll
    for (int m = 1; m < 16; m <<= 1) { a += __shfl_xor(a, m); bb += __shfl_xor(bb, m); }
    a = fsum16(a); bb = fsum16(bb);
    if (r == 0) st8_sc1(ws, WS_L2 + (q * 32 + ti) * 272 + 256 + 8 * h, u32x2{pk2(a, bb), tag});
  }
  STAMP(6);
  {
    float ga = 0.f, gb = 0.f;
    const unsigned gaddr = A_GW + hp * 512 + 16 * h;
#pragma unroll
    for (int s = 0; s < 16; ++s) {
      const u32x4 gv = *(LAS u32x4*)(gaddr + s * 32);
      const u32x4 xv = __builtin_bit_cast(u32x4, hp ? X[16 + s] : X[s]);
#pragma unroll
      for (int d = 0; d < 4; d += 2) {
        const unsigned x0 = xv[d], x1 = xv[d + 1], g0 = gv[d], g1 = gv[d + 1];
        ga = __builtin_amdgcn_fdot2_f32_bf16(__builtin_bit_cast(bf16x2, x0), __builtin_bit_cast(bf16x2, g0), ga, false);
        gb = __builtin_amdgcn_fdot2_f32_bf16(__builtin_bit_cast(bf16x2, x1), __builtin_bit_cast(bf16x2, g1), gb, false);
      }
    }
    *(LAS float*)(A_GP + (32 * th + r) * 16 + (2 * hp + h) * 4) = ga + gb;
  }
  STAMP(7);
  {
    const unsigned o = WS_L2 + (q * 32 + hp * 16 + h) * 272 + (r < 16 ? r : 16) * 16;
    u32x4 g[8];
#pragma unroll 1
  for (int sp = 0; sp < SPIN_CAP; ++sp) {
      asm volatile("" ::: "memory");
      bool ok = true;
#pragma unroll
      for (int i = 0; i < 8; ++i) g[i] = ld16_sc1(ws, o + i * 544);
#pragma unroll
      for (int i = 0; i < 8; ++i) ok &= (g[i][1] == tag) & (g[i][3] == tag);
      if (__all(ok)) break;
      __builtin_amdgcn_s_sleep(2);
    }
    STAMP(8);
    const unsigned oo = WS_O + ((4 * ti + 2 * th + blk) * 16 + 2 * q + hp) * 1024 + (i16 + 16 * h) * 16;
#pragma unroll
    for (int e = 0; e < 2; ++e) {
      const u32x4 f0 = {g[4 * e][0], g[4 * e][2], g[4 * e + 1][0], g[4 * e + 1][2]};
      const u32x4 f1 = {g[4 * e + 2][0], g[4 * e + 2][2], g[4 * e + 3][0], g[4 * e + 3][2]};
      f32x16 Dn = MFMA32(as_frag(f0), pq[e][0], zero16);
      Dn = MFMA32(as_frag(f1), pq[e][1], Dn);
      const float rd = __builtin_amdgcn_rcpf(Dn[8] + 1e-6f);
      const u32x4 ov = {pk2(Dn[0] * rd, Dn[1] * rd), pk2(Dn[2] * rd, Dn[3] * rd), pk2(Dn[4] * rd, Dn[5] * rd), pk2(Dn[6] * rd, Dn[7] * rd)};
      st16_sc1(ws, oo + e * 512, ov);
    }
  }
  wait_vm_bar<0>();
  if (tid == 0) st4_sc1(ws, WS_FO + (ti * 8 + q) * FS, tag);
  STAMP(9);
  const f32x4 gp = *(LAS f32x4*)(A_GP + (16 * w + i16) * 16);
  const float gate = __builtin_amdgcn_rcpf(1.f + __builtin_amdgcn_exp2f(-1.44269504088896341f * ((gp[0] + gp[1]) + (gp[2] + gp[3]))));
  wait_words(ws, lane < 32 ? WS_FY + (q * 32 + lane) * FS : WS_FO + (ti * 8 + (lane & 7)) * FS, tag);
  STAMP(10);
  final_tile(p, ws, ti, q, w, lane, gate, true);
  STAMP(11);
  STAMP_FLUSH();
}

DI f32x2 cmul(f32x2 a, f32x2 b, f32x2 bn) {
  f32x2 ax = {a[0], a[0]}, ay = {a[1], a[1]};
  return __builtin_elementwise_fma(ay, bn, ax * b);
}
DI f32x2 rot(f32x2 b) { return f32x2{-b[1], b[0]}; }
DI f32x2 cexp_l(float lr2, float li2, float l) {
  const float m = __builtin_amdgcn_exp2f(lr2 * l), ph = __builtin_amdgcn_fractf(li2 * l);
  return f32x2{m * __builtin_amdgcn_cosf(ph), m * __builtin_amdgcn_sinf(ph)};
}
DI void put(unsigned a, f32x2 v) { *(LAS unsigned*)a = pk2(v[0], v[1]); }

DI void ssm_block(const Params& p, int sidx) {
  STAMP_DECL;
  STAMP(0);
  const int tid = threadIdx.x, lane = tid & 63, w = __builtin_amdgcn_readfirstlane(tid >> 6);
  const int xcd = sidx & 7, bi = sidx >> 3;
  const int i16 = lane & 15, g = lane >> 4, qq = i16 >> 2, pp = lane & 3;
  const int chl = w >> 1, ch = 64 * xcd + 2 * bi + chl;
  {
    const char* xs = (const char*)p.x + (unsigned)(64 * bi + 16 * w) * 1024u + 128 * xcd;
    const unsigned vo = (lane >> 3) * 1024 + (lane & 7) * 16;
    dma16(xs, vo, S_XT + w * 2048);
    dma16(xs + 8192, vo, S_XT + w * 2048 + 1024);
  }
  const int n = tid & 127, pi = ch * 128 + n;
  const float are = bf2f(p.are[pi]), aim = bf2f(p.aim[pi]), bre = bf2f(p.bre[pi]), bim = bf2f(p.bim[pi]);
  const float cre = bf2f(p.cre[pi]), cim = bf2f(p.cim[pi]);
  const float ldt = bf2f(p.logdt[ch]), dsk = bf2f(p.dskip[ch]);
  const unsigned epoch = load_tag(p.ws + WS_E);
  const unsigned tag = mk_tag(epoch);
  const rsrc_t ws = __builtin_amdgcn_make_buffer_rsrc(p.ws, 0, WS_END, 0x00020000);
  wait_vm_bar<0>();
  STAMP(1);
  {
    const unsigned a = S_XT + (8 * g + qq) * 128 + (16 * w + 4 * pp) * 2;
    const unsigned o = WS_XT + (64 * xcd + 16 * w + i16) * 4096 + (64 * bi + 8 * g) * 2;
    st16_sc1(ws, o, __builtin_bit_cast(u32x4, lds_tr2(a, a + 512)));
    st16_sc1(ws, o + 64, __builtin_bit_cast(u32x4, lds_tr2(a + 4096, a + 4096 + 512)));
  }
  {
    const float dt = (float)(__bf16)expf(ldt);
    const float lr2 = dt * are * 1.44269504088896341f, li2 = dt * aim * 0.15915494309189535f;
    const f32x2 dB = {dt * bre, dt * bim};
    const f32x2 W = cmul(f32x2{cre, cim}, dB, rot(dB));
    const f32x2 E1 = cexp_l(lr2, li2, 1.f), E8 = cexp_l(lr2, li2, 8.f), G = cexp_l(lr2, li2, 256.f);
    const unsigned tx = chl * 2 * TSZ + n * 4, ty = tx + TSZ;
    const f32x2 E1n = rot(E1), Gn = rot(G);
    const f32x2 E2 = cmul(E1, E1, E1n), E2n = rot(E2);
    const f32x2 X1 = cmul(W, E1, E1n), X2 = cmul(W, E2, E2n), X3 = cmul(X1, E2, E2n), X4 = cmul(X2, E2, E2n);
    const f32x2 X5 = cmul(X3, E2, E2n), X6 = cmul(X4, E2, E2n), X7 = cmul(X5, E2, E2n), X8 = cmul(W, E8, rot(E8));
    put(tx + 0 * TROW, W); put(tx + 1 * TROW, X1); put(tx + 2 * TROW, X2); put(tx + 3 * TROW, X3);
    put(tx + 4 * TROW, X4); put(tx + 5 * TROW, X5); put(tx + 6 * TROW, X6); put(tx + 7 * TROW, X7);
    f32x2 A = cmul(W, G, Gn), B = cmul(X8, G, Gn), C = cmul(X4, G, Gn);
    put(tx + 8 * TROW, A); put(tx + 9 * TROW, cmul(X2, G, Gn)); put(tx + 10 * TROW, C);
    put(tx + 12 * TROW, C); put(tx + 13 * TROW, cmul(X6, G, Gn)); put(tx + 14 * TROW, B);
    A = cmul(A, G, Gn); B = cmul(B, G, Gn); C = cmul(C, G, Gn);
    put(tx + 16 * TROW, A); put(tx + 17 * TROW, C); put(tx + 20 * TROW, C); put(tx + 21 * TROW, B);
    A = cmul(A, G, Gn); B = cmul(B, G, Gn); C = cmul(C, G, Gn);
    put(tx + 18 * TROW, A); put(tx + 19 * TROW, C); put(tx + 22 * TROW, C); put(tx + 23 * TROW, B);
#pragma unroll
    for (int m = 0; m < 4; ++m) {
      A = cmul(A, G, Gn); B = cmul(B, G, Gn);
      put(tx + (24 + m) * TROW, A); put(tx + (28 + m) * TROW, B);
    }
    const f32x2 F1 = {E8[0], -E8[1]}, F1n = rot(F1);
    const f32x2 F2 = cmul(F1, F1, F1n), F2n = rot(F2);
    const f32x2 F4 = cmul(F2, F2, F2n), F4n = rot(F4);
    f32x2 y0 = {1.f, 0.f}, y1 = F1, y2 = F2, y3 = cmul(F2, F1, F1n);
    put(ty + 0 * TROW, y0); put(ty + 1 * TROW, y1); put(ty + 2 * TROW, y2); put(ty + 3 * TROW, y3);
    y0 = F4;
#pragma unroll
    for (int m = 1; m < 8; ++m) {
      if (m > 1) y0 = cmul(y0, F4, F4n);
      y1 = cmul(y1, F4, F4n); y2 = cmul(y2, F4, F4n); y3 = cmul(y3, F4, F4n);
      put(ty + (4 * m) * TROW, y0); put(ty + (4 * m + 1) * TROW, y1); put(ty + (4 * m + 2) * TROW, y2); put(ty + (4 * m + 3) * TROW, y3);
    }
  }
  wait_vm_bar<0>();
  if (tid == 0) st4_sc1(ws, WS_FX + (xcd * 32 + bi) * FS, tag);
  STAMP(2);
  if (w < 2) {
    const int r = lane & 31, h = lane >> 5;
    const unsigned ta = w * 2 * TSZ + r * TROW + 16 * h;
    f32x16 v;
#pragma unroll
    for (int i = 0; i < 16; ++i) v[i] = 0.f;
#pragma unroll
    for (int s = 0; s < 16; ++s) v = MFMA32(lds_frag(ta + 32 * s), lds_frag(ta + TSZ + 32 * s), v);
    const unsigned ka = S_KR + w * KSZ + (2044 - 8 * r - 4 * h - 1792) * 2;
    *(LAS u32x2*)(ka + 2 * 1792) = u32x2{pk2(v[3], v[2]), pk2(v[1], v[0])};
    *(LAS u32x2*)(ka + 2 * 1536) = u32x2{pk2(v[6], v[5]), pk2(v[4], v[4])};
    *(LAS u32x2*)(ka + 2 * 1280) = u32x2{pk2(v[9], h ? v[9] : v[8]), pk2(v[8], v[8])};
    *(LAS u32x2*)(ka + 2 * 1024) = u32x2{pk2(v[11], h ? v[11] : v[10]), pk2(v[10], v[10])};
#pragma unroll
    for (int a = 0; a < 4; ++a) {
      const float t = v[12 + a];
      auto sw = __builtin_amdgcn_permlane32_swap(__float_as_uint(t), __float_as_uint(t), false, false);
      const unsigned lo = sw[0];
      const float first = (r & 1) ? t : __uint_as_float(lo);
      *(LAS u32x2*)(ka + 2 * (768 - 256 * a)) = u32x2{pk2(t, t), pk2(t, first)};
    }
  } else if (w == 2) {
    if (lane < 60) *(LAS u32x4*)(S_KR + (lane >= 30 ? KSZ - 480 : 0) + 4096 + lane * 16) = u32x4{0u, 0u, 0u, 0u};
    wait_words(ws, WS_FX + (xcd * 32 + (lane & 31)) * FS, tag);
  }
  bar_lgkm();
  STAMP(3);
  {
    const unsigned o = WS_XT + ch * 4096 + n * 32;
    const u32x4 a = ld16_sc1(ws, o), b = ld16_sc1(ws, o + 16);
    const u32x2 c = ld8_sc1(ws, o + 32);
    const unsigned d[10] = {a[0], a[1], a[2], a[3], b[0], b[1], b[2], b[3], c[0], c[1]};
    unsigned e[9];
#pragma unroll
    for (int k = 0; k < 9; ++k) e[k] = __builtin_amdgcn_alignbit(d[k + 1], d[k], 16);
    const unsigned u = chl * USZ + 32 + n * 32;
    *(LAS u32x4*)(u) = a; *(LAS u32x4*)(u + 16) = b;
    *(LAS u32x4*)(u + UCP) = u32x4{e[0], e[1], e[2], e[3]}; *(LAS u32x4*)(u + UCP + 16) = u32x4{e[4], e[5], e[6], e[7]};
    *(LAS u32x4*)(u + 2 * UCP) = u32x4{d[1], d[2], d[3], d[4]}; *(LAS u32x4*)(u + 2 * UCP + 16) = u32x4{d[5], d[6], d[7], d[8]};
    *(LAS u32x4*)(u + 3 * UCP) = u32x4{e[1], e[2], e[3], e[4]}; *(LAS u32x4*)(u + 3 * UCP + 16) = u32x4{e[5], e[6], e[7], e[8]};
    if (n == 0) {
      const u32x4 z = {0u, 0u, 0u, 0u};
      const unsigned u0 = chl * USZ;
      *(LAS u32x4*)(u0) = z; *(LAS u32x4*)(u0 + 16) = z;
      *(LAS u32x4*)(u0 + UCP) = z; *(LAS u32x4*)(u0 + UCP + 16) = u32x4{0u, 0u, 0u, d[0] << 16};
      *(LAS u32x4*)(u0 + 2 * UCP) = z; *(LAS u32x4*)(u0 + 2 * UCP + 16) = u32x4{0u, 0u, 0u, d[0]};
      *(LAS u32x4*)(u0 + 3 * UCP) = z; *(LAS u32x4*)(u0 + 3 * UCP + 16) = u32x4{0u, 0u, d[0] << 16, e[0]};
    }
  }
  bar_lgkm();
  STAMP(4);
  f32x4 acc[8];
#pragma unroll
  for (int c = 0; c < 8; ++c) acc[c] = f32x4{0.f, 0.f, 0.f, 0.f};
  {
    const int sg = (i16 + 1) & 3;
    unsigned ua = chl * USZ + sg * UCP + (i16 + 1 - sg + 8 * g) * 2 + (w & 1) * 256;
    unsigned kb = S_KR + chl * KSZ + (240 - 16 * i16 + 8 * g) * 2 + (w & 1) * 256;
#pragma unroll 1
    for (int rho = 0; rho < 4; ++rho) {
      bf16x8 A[8], G[8];
#pragma unroll
      for (int m = 0; m < 8; ++m) {
        const u32x2 lo = *(LAS u32x2*)(ua + 512 * m), hi = *(LAS u32x2*)(ua + 512 * m + 8);
        A[m] = as_frag(u32x4{lo[0], lo[1], hi[0], hi[1]});
        G[m] = lds_frag(kb + 512 * (7 - m));
      }
#pragma unroll
      for (int c = 0; c < 8; ++c)
#pragma unroll
        for (int m = 0; m <= c; ++m) acc[c] = MFMA16(A[m], G[c - m], acc[c]);
      ua += 64; kb += 64;
    }
  }
  STAMP(5);
  {
    f32x4 keep[4];
    const unsigned ro = S_RED + w * 4096 + lane * 16, ri = S_RED + (w ^ 1) * 4096 + lane * 16;
    if (w & 1) {
#pragma unroll
      for (int k = 0; k < 4; ++k) { *(LAS f32x4*)(ro + k * 1024) = acc[k]; keep[k] = acc[4 + k]; }
    } else {
#pragma unroll
      for (int k = 0; k < 4; ++k) { *(LAS f32x4*)(ro + k * 1024) = acc[4 + k]; keep[k] = acc[k]; }
    }
    bar_lgkm();
    const int c0 = 4 * (w & 1);
    unsigned o2[4][2];
#pragma unroll
    for (int k = 0; k < 4; ++k) {
      const f32x4 t = *(LAS f32x4*)(ri + k * 1024);
      const u32x2 uu = *(LAS u32x2*)(chl * USZ + (256 * (c0 + k) + 16 * i16 + 4 * g + 16) * 2);
      const unsigned u0 = uu[0], u1 = uu[1];
      o2[k][0] = pk2(keep[k][0] + t[0] + dsk * __uint_as_float(u0 << 16), keep[k][1] + t[1] + dsk * __uint_as_float(u0 & 0xFFFF0000u));
      o2[k][1] = pk2(keep[k][2] + t[2] + dsk * __uint_as_float(u1 << 16), keep[k][3] + t[3] + dsk * __uint_as_float(u1 & 0xFFFF0000u));
    }
#pragma unroll
    for (int k = 0; k < 2; ++k) {
      auto s0 = __builtin_amdgcn_permlane16_swap(o2[2 * k][0], o2[2 * k + 1][0], false, false);
      auto s1 = __builtin_amdgcn_permlane16_swap(o2[2 * k][1], o2[2 * k + 1][1], false, false);
      u32x4 v; v[0] = s0[0]; v[1] = s1[0]; v[2] = s0[1]; v[3] = s1[1];
      const int t = 256 * (c0 + 2 * k + (g & 1)) + 16 * i16 + 8 * (g >> 1);
      st16_sc1(ws, WS_Y + ((t >> 6) * 512 + ch) * 128 + (t & 63) * 2, v);
    }
  }
  wait_vm_bar<0>();
  if (tid == 0) st4_sc1(ws, WS_FY + (xcd * 32 + bi) * FS, tag);
  STAMP(6);
  if (sidx == 0 && w == 0) {
#pragma unroll 1
    for (int k = 0; k < 4; ++k) wait_words(ws, WS_FX + (k * 64 + lane) * FS, tag);
    wait_words(ws, WS_L2 + (lane & 7) * 32 * 272 + 4, tag);
    if (lane == 0) st4_sc1(ws, WS_E, epoch + 1u);
  }
  STAMP(7);
  STAMP_FLUSH();
}

__global__ void __launch_bounds__(256, 2) hh_s4_mix_kernel(Params p) {
  unsigned long long t0_ = __builtin_amdgcn_s_memrealtime();
  const unsigned tg_ = mk_tag(load_tag(p.ws + WS_E));
  const rsrc_t wsr_ = __builtin_amdgcn_make_buffer_rsrc(p.ws, 0, WS_END, 0x00020000);
  if (blockIdx.x == 0 && threadIdx.x == 0) st8_sc1(wsr_, 1024, u32x2{(unsigned)t0_, tg_});
  if (blockIdx.x < 256) att_block(p, blockIdx.x);
  else ssm_block(p, blockIdx.x - 256);
  { wait_words(wsr_, 1028, tg_); unsigned g0 = __builtin_amdgcn_readfirstlane(ld4_sc1(wsr_, 1024));
    for (int i = 0; i < 100000; ++i) { if ((int)((unsigned)__builtin_amdgcn_s_memrealtime() - g0) >= 4000) break; __builtin_amdgcn_s_sleep(1); } }
  if (0) __builtin_amdgcn_s_sleep(1);
}

extern "C" void kernel_launch(void* const* d_in, const int* in_sizes, int n_in, void* d_out, int out_size, void* d_ws,
                              size_t ws_size, hipStream_t stream) {
  Params p;
  p.x = (const u16*)d_in[0]; p.wqkv = (const u16*)d_in[1]; p.wphi = (const u16*)d_in[2]; p.wo = (const u16*)d_in[3];
  p.are = (const u16*)d_in[5]; p.aim = (const u16*)d_in[6]; p.bre = (const u16*)d_in[7]; p.bim = (const u16*)d_in[8];
  p.cre = (const u16*)d_in[9]; p.cim = (const u16*)d_in[10]; p.logdt = (const u16*)d_in[11]; p.dskip = (const u16*)d_in[12];
  p.gw = (const u16*)d_in[13];
  p.out = (u16*)d_out;
  p.ws = (char*)d_ws;
  (void)hipFuncSetAttribute((const void*)hh_s4_mix_kernel, hipFuncAttributeMaxDynamicSharedMemorySize, LDS_BYTES);
  hh_s4_mix_kernel<<<512, 256, LDS_BYTES, stream>>>(p);
}
```

```cpp
#include <hip/hip_runtime.h>
#include <hip/hip_bf16.h>
#include <stdint.h>
#pragma clang diagnostic ignored "-Wint-to-pointer-cast"

#define LAS __attribute__((address_space(3)))
#define GAS __attribute__((address_space(1)))
#define DI __device__ __forceinline__
typedef __bf16 bf16x8 __attribute__((ext_vector_type(8)));
typedef __bf16 bf16x4 __attribute__((ext_vector_type(4)));
typedef __bf16 bf16x2 __attribute__((ext_vector_type(2)));
typedef float f32x16 __attribute__((ext_vector_type(16)));
typedef float f32x4 __attribute__((ext_vector_type(4)));
typedef float f32x2 __attribute__((ext_vector_type(2)));
typedef unsigned u32x4 __attribute__((ext_vector_type(4)));
typedef unsigned u32x2 __attribute__((ext_vector_type(2)));
typedef unsigned short u16;

#define FUSED 1
#define WARM 0

constexpr unsigned WS_E = 0;
constexpr unsigned FS = 128;
constexpr unsigned WS_FX = 4096;
constexpr unsigned WS_FY = WS_FX + 256 * FS;
constexpr unsigned WS_FO = WS_FY + 256 * FS;
constexpr unsigned WS_L2 = WS_FO + 256 * FS;
constexpr unsigned WS_L1Z = WS_L2 + 8 * 32 * 272;
constexpr unsigned WS_L1K = WS_L1Z + 8 * 32 * 512;
constexpr unsigned WS_XT = WS_L1K + 8 * 32 * 32 * 256;
constexpr unsigned WS_Y = WS_XT + 512 * 4096 + 4096;
constexpr unsigned WS_O = WS_Y + 32 * 512 * 128;
constexpr unsigned WS_ST = WS_O + 128 * 16 * 1024;
constexpr unsigned WS_END = WS_ST + 512 * 4 * 16 * 8;

constexpr unsigned SLOT = 24576;
constexpr unsigned A_XCH = SLOT + 16384;
constexpr unsigned A_WPHI = 3 * SLOT;
constexpr unsigned A_GW = A_WPHI + 2048;
constexpr unsigned A_XZ = A_GW + 1024;
constexpr unsigned A_GP = A_XZ + 512;
constexpr unsigned A_DUMMY = A_GP + 1024;
constexpr unsigned A_WO = 0, A_Y = 65536;
constexpr unsigned TROW = 528, TSZ = 32 * TROW;
constexpr unsigned S_KR = 4 * TSZ, KSZ = 4608;
constexpr unsigned S_XT = S_KR;
constexpr unsigned UCP = 4160, USZ = 4 * UCP;
constexpr unsigned S_RED = 2 * USZ;
constexpr unsigned LDS_BYTES = 79872;
constexpr int SPIN_CAP = 1 << 17;

struct Params {
  const u16 *x, *wqkv, *wphi, *wo, *are, *aim, *bre, *bim, *cre, *cim, *logdt, *dskip, *gw;
  u16* out;
  char* ws;
};

DI unsigned pk2(float a, float b) { bf16x2 v; v[0] = (__bf16)a; v[1] = (__bf16)b; return __builtin_bit_cast(unsigned, v); }
DI float bf2f(u16 h) { return __uint_as_float((unsigned)h << 16); }
DI bf16x8 as_frag(u32x4 v) { return __builtin_bit_cast(bf16x8, v); }
DI bf16x8 pack8(const f32x16& a, int s) {
  u32x4 p;
  p[0] = pk2(a[8 * s + 0], a[8 * s + 1]); p[1] = pk2(a[8 * s + 2], a[8 * s + 3]);
  p[2] = pk2(a[8 * s + 4], a[8 * s + 5]); p[3] = pk2(a[8 * s + 6], a[8 * s + 7]);
  return as_frag(p);
}
DI void dma16(const void* sbase, unsigned voff, unsigned lds) {
  asm volatile("s_mov_b32 m0, %2\n\ts_nop 0\n\tglobal_load_lds_dwordx4 %0, %1" ::"v"(voff), "s"(sbase), "s"(lds) : "memory");
}
DI void dma16_sc1(const void* sbase, unsigned voff, unsigned lds) {
  asm volatile("s_mov_b32 m0, %2\n\ts_nop 0\n\tglobal_load_lds_dwordx4 %0, %1 sc1" ::"v"(voff), "s"(sbase), "s"(lds) : "memory");
}
DI void dma4(const void* sbase, unsigned voff, unsigned lds) {
  asm volatile("s_mov_b32 m0, %2\n\ts_nop 0\n\tglobal_load_lds_dword %0, %1" ::"v"(voff), "s"(sbase), "s"(lds) : "memory");
}
template <int N> DI void wait_vm_bar() { asm volatile("s_waitcnt vmcnt(%0) lgkmcnt(0)\n\ts_barrier" ::"n"(N) : "memory"); }
DI void bar_lgkm() { asm volatile("s_waitcnt lgkmcnt(0)\n\ts_barrier" ::: "memory"); }
DI bf16x8 lds_frag(unsigned a) { return *(LAS bf16x8*)a; }
DI bf16x4 lds_tr(unsigned a) { return __builtin_amdgcn_ds_read_tr16_b64_v4bf16((LAS bf16x4*)a); }
DI bf16x8 lds_tr2(unsigned a0, unsigned a1) { return __builtin_shufflevector(lds_tr(a0), lds_tr(a1), 0, 1, 2, 3, 4, 5, 6, 7); }
#define MFMA32(a, b, c) __builtin_amdgcn_mfma_f32_32x32x16_bf16((a), (b), (c), 0, 0, 0)
#define MFMA16(a, b, c) __builtin_amdgcn_mfma_f32_16x16x32_bf16((a), (b), (c), 0, 0, 0)
DI float fsum32(float a) {
  auto r = __builtin_amdgcn_permlane32_swap(__float_as_uint(a), __float_as_uint(a), false, false);
  unsigned r0 = r[0], r1 = r[1];
  return __uint_as_float(r0) + __uint_as_float(r1);
}
DI float fmax32(float a) {
  auto r = __builtin_amdgcn_permlane32_swap(__float_as_uint(a), __float_as_uint(a), false, false);
  unsigned r0 = r[0], r1 = r[1];
  return fmaxf(__uint_as_float(r0), __uint_as_float(r1));
}
DI float fmin32(float a) {
  auto r = __builtin_amdgcn_permlane32_swap(__float_as_uint(a), __float_as_uint(a), false, false);
  unsigned r0 = r[0], r1 = r[1];
  return fminf(__uint_as_float(r0), __uint_as_float(r1));
}
DI float fsum16(float a) {
  auto r = __builtin_amdgcn_permlane16_swap(__float_as_uint(a), __float_as_uint(a), false, false);
  unsigned r0 = r[0], r1 = r[1];
  return __uint_as_float(r0) + __uint_as_float(r1);
}
DI unsigned load_tag(const char* ws) {
  unsigned e;
  asm volatile("s_load_dword %0, %1, 0x0\n\ts_waitcnt lgkmcnt(0)" : "=s"(e) : "s"(ws) : "memory");
  return e;
}
DI unsigned mk_tag(unsigned e) { return (e + 1u) * 0x9E3779B1u + 0x7FEB352Du; }
typedef __amdgpu_buffer_rsrc_t rsrc_t;
DI u32x4 ld16_sc1(rsrc_t r, unsigned off) { return __builtin_amdgcn_raw_buffer_load_b128(r, off, 0, 16); }
DI u32x2 ld8_sc1(rsrc_t r, unsigned off) { return __builtin_amdgcn_raw_buffer_load_b64(r, off, 0, 16); }
DI unsigned ld4_sc1(rsrc_t r, unsigned off) { return __builtin_amdgcn_raw_buffer_load_b32(r, off, 0, 16); }
DI void st16_sc1(rsrc_t r, unsigned off, u32x4 v) { __builtin_amdgcn_raw_buffer_store_b128(v, r, off, 0, 16); }
DI void st8_sc1(rsrc_t r, unsigned off, u32x2 v) { __builtin_amdgcn_raw_buffer_store_b64(v, r, off, 0, 16); }
DI void st4_sc1(rsrc_t r, unsigned off, unsigned v) { __builtin_amdgcn_raw_buffer_store_b32(v, r, off, 0, 16); }
DI void wait_words(rsrc_t r, unsigned off, unsigned tag) {
#pragma unroll 1
  for (int sp = 0; sp < SPIN_CAP; ++sp) {
    asm volatile("" ::: "memory");
    unsigned f = ld4_sc1(r, off);
    if (__all(f == tag)) break;
    __builtin_amdgcn_s_sleep(2);
  }
  asm volatile("" ::: "memory");
}

#define STAMP(k)
#define STAMP_DECL
#define STAMP_FLUSH()

DI void dual_softmax(const f32x16& P, int e, bf16x8& fp, bf16x8& fm) {
  const float L = 1.44269504088896341f;
  float v[8];
#pragma unroll
  for (int j = 0; j < 8; ++j) v[j] = P[8 * e + j] * L;
  float mx = fmaxf(fmaxf(fmaxf(v[0], v[1]), fmaxf(v[2], v[3])), fmaxf(fmaxf(v[4], v[5]), fmaxf(v[6], v[7])));
  float mn = fminf(fminf(fminf(v[0], v[1]), fminf(v[2], v[3])), fminf(fminf(v[4], v[5]), fminf(v[6], v[7])));
  mx = fmax32(mx); mn = fmin32(mn);
  float a[8], b[8];
#pragma unroll
  for (int j = 0; j < 8; ++j) { a[j] = __builtin_amdgcn_exp2f(v[j] - mx); b[j] = __builtin_amdgcn_exp2f(mn - v[j]); }
  float sa = ((a[0] + a[1]) + (a[2] + a[3])) + ((a[4] + a[5]) + (a[6] + a[7]));
  float sb = ((b[0] + b[1]) + (b[2] + b[3])) + ((b[4] + b[5]) + (b[6] + b[7]));
  float ra = __builtin_amdgcn_rcpf(fsum32(sa)), rb = __builtin_amdgcn_rcpf(fsum32(sb));
  u32x4 x, y;
#pragma unroll
  for (int j = 0; j < 4; ++j) { x[j] = pk2(a[2 * j] * ra, a[2 * j + 1] * ra); y[j] = pk2(b[2 * j] * rb, b[2 * j + 1] * rb); }
  fp = as_frag(x); fm = as_frag(y);
}

DI void final_tile(const Params& p, rsrc_t ws, int ti, int q, int w, int lane, float gate, bool sc1) {
  const int i16 = lane & 15, g = lane >> 4, qq = i16 >> 2, pp = lane & 3;
  {
    const char* ysrc = p.ws + WS_Y + (unsigned)(ti * 512 + 64 * q + 16 * w) * 128u;
    const int rr = lane >> 3;
    const unsigned vo = rr * 128 + (((lane & 7) ^ (((rr >> 1) & 3) << 1)) << 4);
    dma16_sc1(ysrc, vo, A_Y + w * 2048);
    dma16_sc1(ysrc + 1024, vo, A_Y + w * 2048 + 1024);
  }
  u32x4 of[16];
  const unsigned obase = WS_O + (unsigned)(4 * ti + w) * 16384u + lane * 16;
#pragma unroll
  for (int k = 0; k < 16; ++k) of[k] = ld16_sc1(ws, obase + k * 1024);
  const int key = (2 * (g & 1) + (qq >> 1)) & 3;
  unsigned abase[4];
#pragma unroll
  for (int ct = 0; ct < 4; ++ct)
    abase[ct] = A_WO + (16 * (g >> 1) + 4 * (g & 1) + qq) * 128 + (((2 * ct + (pp >> 1)) ^ (key << 1)) << 4) + 8 * (pp & 1);
  f32x4 acc[4];
#pragma unroll
  for (int ct = 0; ct < 4; ++ct) acc[ct] = f32x4{0.f, 0.f, 0.f, 0.f};
#pragma unroll
  for (int k = 0; k < 16; ++k) {
#pragma unroll
    for (int ct = 0; ct < 4; ++ct) {
      bf16x8 a = lds_tr2(abase[ct] + k * 4096, abase[ct] + k * 4096 + 1024);
      acc[ct] = MFMA16(a, as_frag(of[k]), acc[ct]);
    }
  }
  wait_vm_bar<0>();
  const int ykey = (2 * g + (qq >> 1)) & 3;
  const unsigned ybase = A_Y + (4 * g + qq) * 128 + (((2 * w + (pp >> 1)) ^ (ykey << 1)) << 4) + 8 * (pp & 1);
  unsigned o2[4][2];
#pragma unroll
  for (int ct = 0; ct < 4; ++ct) {
    bf16x4 yb = lds_tr(ybase + ct * 2048);
    float r[4];
#pragma unroll
    for (int j = 0; j < 4; ++j) { float b = (float)yb[j]; r[j] = b + gate * (acc[ct][j] - b); }
    o2[ct][0] = pk2(r[0], r[1]); o2[ct][1] = pk2(r[2], r[3]);
  }
  char* orow = (char*)p.out + (unsigned)(64 * ti + 16 * w + i16) * 1024u + 128 * q + 32 * (g & 1) + 16 * (g >> 1);
#pragma unroll
  for (int c2 = 0; c2 < 2; ++c2) {
    auto s0 = __builtin_amdgcn_permlane16_swap(o2[2 * c2][0], o2[2 * c2 + 1][0], false, false);
    auto s1 = __builtin_amdgcn_permlane16_swap(o2[2 * c2][1], o2[2 * c2 + 1][1], false, false);
    u32x4 v; v[0] = s0[0]; v[1] = s1[0]; v[2] = s0[1]; v[3] = s1[1];
    *(u32x4*)(orow + 64 * c2) = v;
  }
}

DI void wo_dma(const Params& p, int q, int part, int nparts, int lane) {
  const int rr = lane >> 3;
  const unsigned vo = rr * 1024 + (((lane & 7) ^ (((rr >> 1) & 3) << 1)) << 4);
  const int n = 64 / nparts;
  const char* src = (const char*)p.wo + 128 * q + (unsigned)(part * n) * 8192u;
  unsigned dst = A_WO + part * n * 1024;
#pragma unroll 4
  for (int i = 0; i < n; ++i) dma16(src + i * 8192, vo, dst + i * 1024);
}

DI void att_block(const Params& p, int b) {
  STAMP_DECL;
  STAMP(0);
  const int tid = threadIdx.x, lane = tid & 63, w = __builtin_amdgcn_readfirstlane(tid >> 6);
  const int xcd = b & 7, bi = b >> 3, ti = 4 * xcd + (bi & 3), q = bi >> 2;
  const int th = w & 1, hp = w >> 1;
  const int r = lane & 31, h = lane >> 5, blk = (lane >> 4) & 1, i16 = lane & 15, qq = i16 >> 2, pp = lane & 3;
  __builtin_amdgcn_s_setprio(2);
  const int rr = lane >> 3, sl = lane & 7;
  const unsigned vx0 = rr * 1024 + ((sl ^ (rr >> 1)) << 4), vx1 = rr * 1024 + ((sl ^ (4 + (rr >> 1))) << 4);
  const unsigned vw = rr * 3072 + ((sl ^ (((rr >> 1) & 1) << 2)) << 4);
  const char* xs = (const char*)p.x + (unsigned)(64 * ti + 16 * w) * 1024u;
  const char* wsrc = (const char*)p.wqkv + (unsigned)(16 * w) * 3072u + 128 * q;
  const unsigned ldw = w * 2048;
  auto stage_kv = [&](int c, unsigned slot) {
    dma16(xs + c * 128, vx0, slot + ldw);
    dma16(xs + c * 128 + 8192, vx1, slot + ldw + 1024);
    dma16(wsrc + c * 196608 + 1024, vw, slot + 8192 + ldw);
    dma16(wsrc + c * 196608 + 1024 + 24576, vw, slot + 8192 + ldw + 1024);
    dma16(wsrc + c * 196608 + 2048, vw, slot + 16384 + ldw);
    dma16(wsrc + c * 196608 + 2048 + 24576, vw, slot + 16384 + ldw + 1024);
  };
  auto stage_q = [&](int c0, int n, unsigned slot) {
    for (int i = 0; i < n; ++i) {
      dma16(wsrc + (c0 + i) * 196608, vw, slot + i * 8192 + ldw);
      dma16(wsrc + (c0 + i) * 196608 + 24576, vw, slot + i * 8192 + ldw + 1024);
    }
  };
  if (w < 2) dma16((const char*)p.wphi + q * 2048 + w * 1024, lane * 16, A_WPHI + w * 1024);
  else if (w == 2) dma16((const char*)p.gw, lane * 16, A_GW);
  stage_kv(0, 0); stage_kv(1, SLOT); stage_kv(2, 2 * SLOT);
  constexpr int NW = 0;
  const unsigned tag = mk_tag(load_tag(p.ws + WS_E));
  const rsrc_t ws = __builtin_amdgcn_make_buffer_rsrc(p.ws, 0, WS_END, 0x00020000);

  unsigned xa[4];
#pragma unroll
  for (int s = 0; s < 4; ++s) xa[s] = (32 * th + r) * 128 + (((2 * s + h) ^ ((r >> 1) & 7)) << 4);
  const unsigned wa = (8 * h + qq) * 128 + (((4 * hp + 2 * blk + (pp >> 1)) ^ ((qq >> 1) << 2)) << 4) + 8 * (pp & 1);

  bf16x8 X[32];
  f32x16 ak, av;
#pragma unroll
  for (int i = 0; i < 16; ++i) { ak[i] = 0.f; av[i] = 0.f; }
#pragma unroll
  for (int c = 0; c < 8; ++c) {
    const unsigned slot = (c % 3) * SLOT;
    if (c < 3) wait_vm_bar<12 + NW>(); else wait_vm_bar<12>();
    if (c == 0) STAMP(1);
    bf16x8 wk[4], wv[4];
#pragma unroll
    for (int s = 0; s < 4; ++s) {
      X[4 * c + s] = lds_frag(slot + xa[s]);
      wk[s] = lds_tr2(slot + 8192 + wa + s * 2048, slot + 8192 + wa + s * 2048 + 512);
      wv[s] = lds_tr2(slot + 16384 + wa + s * 2048, slot + 16384 + wa + s * 2048 + 512);
    }
    bar_lgkm();
    if (c < 5) stage_kv(c + 3, slot);
    else if (c == 5) stage_q(0, 3, slot);
    else if (c == 6) stage_q(3, 3, slot);
    else stage_q(6, 2, slot);
#pragma unroll
    for (int s = 0; s < 4; ++s) {
      ak = MFMA32(wk[s], X[4 * c + s], ak);
      av = MFMA32(X[4 * c + s], wv[s], av);
    }
  }
  STAMP(2);
  bf16x8 A1, A2, SEL0, SEL1;
  {
    const unsigned a = A_WPHI + (2 * hp + blk) * 512 + (4 * h + qq) * 32 + 8 * pp;
    u32x4 wf = __builtin_bit_cast(u32x4, lds_tr2(a, a + 256));
    u32x4 z = {0u, 0u, 0u, 0u};
    A1 = as_frag(blk ? z : wf);
    A2 = as_frag(blk ? wf : z);
    u32x4 s0, s1;
#pragma unroll
    for (int d = 0; d < 4; ++d) {
      const int f0 = ((2 * d) & 3) + 8 * (d >> 1) + 4 * h;
      s0[d] = (r == f0 ? 0x3F80u : 0u) | (r == f0 + 1 ? 0x3F800000u : 0u);
      s1[d] = (r == f0 + 16 ? 0x3F80u : 0u) | (r == f0 + 17 ? 0x3F800000u : 0u);
    }
    SEL0 = as_frag(s0); SEL1 = as_frag(s1);
  }
  f32x16 zero16;
#pragma unroll
  for (int i = 0; i < 16; ++i) zero16[i] = 0.f;
  f32x16 R, Z;
  {
    f32x16 P = MFMA32(A1, pack8(ak, 0), zero16);
    P = MFMA32(A2, pack8(ak, 1), P);
    const u32x4 ones = {0x3F803F80u, 0x3F803F80u, 0x3F803F80u, 0x3F803F80u};
    const u32x4 v0 = __builtin_bit_cast(u32x4, pack8(av, 0)), v1 = __builtin_bit_cast(u32x4, pack8(av, 1));
    f32x16 Dh[2];
#pragma unroll
    for (int e = 0; e < 2; ++e) {
      bf16x8 fp, fm;
      dual_softmax(P, e, fp, fm);
      f32x16 T = MFMA32(fp, SEL0, zero16);
      T = MFMA32(fm, SEL1, T);
      const bool mine = (blk == e);
      Dh[e] = MFMA32(pack8(T, 0), as_frag(mine ? v0 : ones), zero16);
      Dh[e] = MFMA32(pack8(T, 1), as_frag(mine ? v1 : ones), Dh[e]);
    }
#pragma unroll
    for (int i = 0; i < 16; ++i) { R[i] = blk ? Dh[1][i] : Dh[0][i]; Z[i] = blk ? Dh[0][i] : Dh[1][i]; }
  }
  {
    float kr[8], kz[8];
    f32x4 s0, s1, z0, z1;
#pragma unroll
    for (int j = 0; j < 4; ++j) {
      s0[j] = th ? R[j] : R[8 + j]; s1[j] = th ? R[4 + j] : R[12 + j];
      z0[j] = th ? Z[j] : Z[8 + j]; z1[j] = th ? Z[4 + j] : Z[12 + j];
      kr[j] = th ? R[8 + j] : R[j]; kr[4 + j] = th ? R[12 + j] : R[4 + j];
      kz[j] = th ? Z[8 + j] : Z[j]; kz[4 + j] = th ? Z[12 + j] : Z[4 + j];
    }
    *(LAS f32x4*)(A_XCH + w * 2048 + lane * 16) = s0;
    *(LAS f32x4*)(A_XCH + w * 2048 + 1024 + lane * 16) = s1;
    if (i16 == 0) {
      *(LAS f32x4*)(A_XZ + w * 128 + (lane >> 4) * 32) = z0;
      *(LAS f32x4*)(A_XZ + w * 128 + (lane >> 4) * 32 + 16) = z1;
    }
    wait_vm_bar<0>();
    const f32x4 t0 = *(LAS f32x4*)(A_XCH + (w ^ 1) * 2048 + lane * 16), t1 = *(LAS f32x4*)(A_XCH + (w ^ 1) * 2048 + 1024 + lane * 16);
    const f32x4 y0 = *(LAS f32x4*)(A_XZ + (w ^ 1) * 128 + (lane >> 4) * 32), y1 = *(LAS f32x4*)(A_XZ + (w ^ 1) * 128 + (lane >> 4) * 32 + 16);
    const unsigned slk = (((2 * hp + blk) * 2 + th) * 2) * 2 + h;
    const unsigned slz = (((2 * hp + 1 - blk) * 2 + th) * 2) * 2 + h;
    const unsigned ok = WS_L1K + ((q * 32 + slk) * 32 + ti) * 256 + i16 * 16;
    st16_sc1(ws, ok, u32x4{pk2(kr[0] + t0[0], kr[1] + t0[1]), tag, pk2(kr[2] + t0[2], kr[3] + t0[3]), tag});
    st16_sc1(ws, ok + 2 * 32 * 256, u32x4{pk2(kr[4] + t1[0], kr[5] + t1[1]), tag, pk2(kr[6] + t1[2], kr[7] + t1[3]), tag});
    if (i16 == 0) {
      const unsigned oz = WS_L1Z + ((q * 32 + slz) * 32 + ti) * 16;
      st16_sc1(ws, oz, u32x4{pk2(kz[0] + y0[0], kz[1] + y0[1]), tag, pk2(kz[2] + y0[2], kz[3] + y0[3]), tag});
      st16_sc1(ws, oz + 2 * 32 * 16, u32x4{pk2(kz[4] + y1[0], kz[5] + y1[1]), tag, pk2(kz[6] + y1[2], kz[7] + y1[3]), tag});
    }
  }
  STAMP(3);
  bf16x8 pq[2][2];
  {
    f32x16 aq = zero16;
#pragma unroll
    for (int c = 0; c < 8; ++c) {
      const unsigned base = (c < 3 ? 2 * SLOT + c * 8192 : c < 6 ? (c - 3) * 8192 : SLOT + (c - 6) * 8192) + wa;
#pragma unroll
      for (int s = 0; s < 4; ++s) aq = MFMA32(lds_tr2(base + s * 2048, base + s * 2048 + 512), X[4 * c + s], aq);
    }
    bar_lgkm();
    STAMP(4);
    if (w >= 2) wo_dma(p, q, w - 2, 2, lane);
    f32x16 P = MFMA32(A1, pack8(aq, 0), zero16);
    P = MFMA32(A2, pack8(aq, 1), P);
    dual_softmax(P, 0, pq[0][0], pq[0][1]);
    dual_softmax(P, 1, pq[1][0], pq[1][1]);
  }
  STAMP(5);
  unsigned lo_bits = 0x00003F80u, hi_bits = 0x3F800000u;
  asm volatile("" : "+s"(lo_bits), "+s"(hi_bits));
  const bf16x2 LO = __builtin_bit_cast(bf16x2, lo_bits), HI = __builtin_bit_cast(bf16x2, hi_bits);
  if (w == 0) {
    const unsigned o = WS_L1K + ((q * 32 + ti) * 32 + (lane >> 4) * 8) * 256 + i16 * 16;
    u32x4 g[8];
#pragma unroll 1
  for (int sp = 0; sp < SPIN_CAP; ++sp) {
      asm volatile("" ::: "memory");
      bool ok = true;
#pragma unroll
      for (int i = 0; i < 8; ++i) g[i] = ld16_sc1(ws, o + i * 256);
#pragma unroll
      for (int i = 0; i < 8; ++i) ok &= (g[i][1] == tag) & (g[i][3] == tag);
      if (__all(ok)) break;
      __builtin_amdgcn_s_sleep(2);
    }
    float a = 0.f, bb = 0.f, c = 0.f, d = 0.f;
#pragma unroll
    for (int i = 0; i < 8; ++i) {
      const unsigned p0 = g[i][0], p1 = g[i][2];
      a = __builtin_amdgcn_fdot2_f32_bf16(__builtin_bit_cast(bf16x2, p0), LO, a, false);
      bb = __builtin_amdgcn_fdot2_f32_bf16(__builtin_bit_cast(bf16x2, p0), HI, bb, false);
      c = __builtin_amdgcn_fdot2_f32_bf16(__builtin_bit_cast(bf16x2, p1), LO, c, false);
      d = __builtin_amdgcn_fdot2_f32_bf16(__builtin_bit_cast(bf16x2, p1), HI, d, false);
    }
    auto ac = __builtin_amdgcn_permlane32_swap(__float_as_uint(a), __float_as_uint(c), false, false);
    auto bd = __builtin_amdgcn_permlane32_swap(__float_as_uint(bb), __float_as_uint(d), false, false);
    const unsigned ac0 = ac[0], ac1 = ac[1], bd0 = bd[0], bd1 = bd[1];
    const float s1 = fsum16(__uint_as_float(ac0) + __uint_as_float(ac1)), s2 = fsum16(__uint_as_float(bd0) + __uint_as_float(bd1));
    if (blk == 0) st8_sc1(ws, WS_L2 + (q * 32 + ti) * 272 + i16 * 16 + 8 * h, u32x2{pk2(s1, s2), tag});
  } else if (w == 1) {
    const unsigned o = WS_L1Z + ((q * 32 + ti) * 32 + r) * 16;
    u32x4 g;
#pragma unroll 1
  for (int sp = 0; sp < SPIN_CAP; ++sp) {
      asm volatile("" ::: "memory");
      g = ld16_sc1(ws, o);
      if (__all((g[1] == tag) & (g[3] == tag))) break;
      __builtin_amdgcn_s_sleep(2);
    }
    const unsigned pw = h ? g[2] : g[0];
    float a = __uint_as_float(pw << 16), bb = __uint_as_float(pw & 0xFFFF0000u);
#pragma unroll
    for (int m = 1; m < 16; m <<= 1) { a += __shfl_xor(a, m); bb += __shfl_xor(bb, m); }
    a = fsum16(a); bb = fsum16(bb);
    if (r == 0) st8_sc1(ws, WS_L2 + (q * 32 + ti) * 272 + 256 + 8 * h, u32x2{pk2(a, bb), tag});
  }
  STAMP(6);
  {
    float ga = 0.f, gb = 0.f;
    const unsigned gaddr = A_GW + hp * 512 + 16 * h;
#pragma unroll
    for (int s = 0; s < 16; ++s) {
      const u32x4 gv = *(LAS u32x4*)(gaddr + s * 32);
      const u32x4 xv = __builtin_bit_cast(u32x4, hp ? X[16 + s] : X[s]);
#pragma unroll
      for (int d = 0; d < 4; d += 2) {
        const unsigned x0 = xv[d], x1 = xv[d + 1], g0 = gv[d], g1 = gv[d + 1];
        ga = __builtin_amdgcn_fdot2_f32_bf16(__builtin_bit_cast(bf16x2, x0), __builtin_bit_cast(bf16x2, g0), ga, false);
        gb = __builtin_amdgcn_fdot2_f32_bf16(__builtin_bit_cast(bf16x2, x1), __builtin_bit_cast(bf16x2, g1), gb, false);
      }
    }
    *(LAS float*)(A_GP + (32 * th + r) * 16 + (2 * hp + h) * 4) = ga + gb;
  }
  STAMP(7);
  {
    const unsigned o = WS_L2 + (q * 32 + hp * 16 + h) * 272 + (r < 16 ? r : 16) * 16;
    u32x4 g[8];
#pragma unroll 1
  for (int sp = 0; sp < SPIN_CAP; ++sp) {
      asm volatile("" ::: "memory");
      bool ok = true;
#pragma unroll
      for (int i = 0; i < 8; ++i) g[i] = ld16_sc1(ws, o + i * 544);
#pragma unroll
      for (int i = 0; i < 8; ++i) ok &= (g[i][1] == tag) & (g[i][3] == tag);
      if (__all(ok)) break;
      __builtin_amdgcn_s_sleep(2);
    }
    STAMP(8);
    const unsigned oo = WS_O + ((4 * ti + 2 * th + blk) * 16 + 2 * q + hp) * 1024 + (i16 + 16 * h) * 16;
#pragma unroll
    for (int e = 0; e < 2; ++e) {
      const u32x4 f0 = {g[4 * e][0], g[4 * e][2], g[4 * e + 1][0], g[4 * e + 1][2]};
      const u32x4 f1 = {g[4 * e + 2][0], g[4 * e + 2][2], g[4 * e + 3][0], g[4 * e + 3][2]};
      f32x16 Dn = MFMA32(as_frag(f0), pq[e][0], zero16);
      Dn = MFMA32(as_frag(f1), pq[e][1], Dn);
      const float rd = __builtin_amdgcn_rcpf(Dn[8] + 1e-6f);
      const u32x4 ov = {pk2(Dn[0] * rd, Dn[1] * rd), pk2(Dn[2] * rd, Dn[3] * rd), pk2(Dn[4] * rd, Dn[5] * rd), pk2(Dn[6] * rd, Dn[7] * rd)};
      st16_sc1(ws, oo + e * 512, ov);
    }
  }
  wait_vm_bar<0>();
  if (tid == 0) st4_sc1(ws, WS_FO + (ti * 8 + q) * FS, tag);
  STAMP(9);
  const f32x4 gp = *(LAS f32x4*)(A_GP + (16 * w + i16) * 16);
  const float gate = __builtin_amdgcn_rcpf(1.f + __builtin_amdgcn_exp2f(-1.44269504088896341f * ((gp[0] + gp[1]) + (gp[2] + gp[3]))));
  wait_words(ws, lane < 32 ? WS_FY + (q * 32 + lane) * FS : WS_FO + (ti * 8 + (lane & 7)) * FS, tag);
  STAMP(10);
  final_tile(p, ws, ti, q, w, lane, gate, true);
  STAMP(11);
  STAMP_FLUSH();
}

DI f32x2 cmul(f32x2 a, f32x2 b, f32x2 bn) {
  f32x2 ax = {a[0], a[0]}, ay = {a[1], a[1]};
  return __builtin_elementwise_fma(ay, bn, ax * b);
}
DI f32x2 rot(f32x2 b) { return f32x2{-b[1], b[0]}; }
DI f32x2 cexp_l(float lr2, float li2, float l) {
  const float m = __builtin_amdgcn_exp2f(lr2 * l), ph = __builtin_amdgcn_fractf(li2 * l);
  return f32x2{m * __builtin_amdgcn_cosf(ph), m * __builtin_amdgcn_sinf(ph)};
}
DI void put(unsigned a, f32x2 v) { *(LAS unsigned*)a = pk2(v[0], v[1]); }

DI void ssm_block(const Params& p, int sidx) {
  STAMP_DECL;
  STAMP(0);
  const int tid = threadIdx.x, lane = tid & 63, w = __builtin_amdgcn_readfirstlane(tid >> 6);
  const int xcd = sidx & 7, bi = sidx >> 3;
  const int i16 = lane & 15, g = lane >> 4, qq = i16 >> 2, pp = lane & 3;
  const int chl = w >> 1, ch = 64 * xcd + 2 * bi + chl;
  {
    const char* xs = (const char*)p.x + (unsigned)(64 * bi + 16 * w) * 1024u + 128 * xcd;
    const unsigned vo = (lane >> 3) * 1024 + (lane & 7) * 16;
    dma16(xs, vo, S_XT + w * 2048);
    dma16(xs + 8192, vo, S_XT + w * 2048 + 1024);
  }
  const int n = tid & 127, pi = ch * 128 + n;
  const float are = bf2f(p.are[pi]), aim = bf2f(p.aim[pi]), bre = bf2f(p.bre[pi]), bim = bf2f(p.bim[pi]);
  const float cre = bf2f(p.cre[pi]), cim = bf2f(p.cim[pi]);
  const float ldt = bf2f(p.logdt[ch]), dsk = bf2f(p.dskip[ch]);
  const unsigned epoch = load_tag(p.ws + WS_E);
  const unsigned tag = mk_tag(epoch);
  const rsrc_t ws = __builtin_amdgcn_make_buffer_rsrc(p.ws, 0, WS_END, 0x00020000);
  wait_vm_bar<0>();
  STAMP(1);
  {
    const unsigned a = S_XT + (8 * g + qq) * 128 + (16 * w + 4 * pp) * 2;
    const unsigned o = WS_XT + (64 * xcd + 16 * w + i16) * 4096 + (64 * bi + 8 * g) * 2;
    st16_sc1(ws, o, __builtin_bit_cast(u32x4, lds_tr2(a, a + 512)));
    st16_sc1(ws, o + 64, __builtin_bit_cast(u32x4, lds_tr2(a + 4096, a + 4096 + 512)));
  }
  {
    const float dt = (float)(__bf16)expf(ldt);
    const float lr2 = dt * are * 1.44269504088896341f, li2 = dt * aim * 0.15915494309189535f;
    const f32x2 dB = {dt * bre, dt * bim};
    const f32x2 W = cmul(f32x2{cre, cim}, dB, rot(dB));
    const f32x2 E1 = cexp_l(lr2, li2, 1.f), E8 = cexp_l(lr2, li2, 8.f), G = cexp_l(lr2, li2, 256.f);
    const unsigned tx = chl * 2 * TSZ + n * 4, ty = tx + TSZ;
    const f32x2 E1n = rot(E1), Gn = rot(G);
    const f32x2 E2 = cmul(E1, E1, E1n), E2n = rot(E2);
    const f32x2 X1 = cmul(W, E1, E1n), X2 = cmul(W, E2, E2n), X3 = cmul(X1, E2, E2n), X4 = cmul(X2, E2, E2n);
    const f32x2 X5 = cmul(X3, E2, E2n), X6 = cmul(X4, E2, E2n), X7 = cmul(X5, E2, E2n), X8 = cmul(W, E8, rot(E8));
    put(tx + 0 * TROW, W); put(tx + 1 * TROW, X1); put(tx + 2 * TROW, X2); put(tx + 3 * TROW, X3);
    put(tx + 4 * TROW, X4); put(tx + 5 * TROW, X5); put(tx + 6 * TROW, X6); put(tx + 7 * TROW, X7);
    f32x2 A = cmul(W, G, Gn), B = cmul(X8, G, Gn), C = cmul(X4, G, Gn);
    put(tx + 8 * TROW, A); put(tx + 9 * TROW, cmul(X2, G, Gn)); put(tx + 10 * TROW, C);
    put(tx + 12 * TROW, C); put(tx + 13 * TROW, cmul(X6, G, Gn)); put(tx + 14 * TROW, B);
    A = cmul(A, G, Gn); B = cmul(B, G, Gn); C = cmul(C, G, Gn);
    put(tx + 16 * TROW, A); put(tx + 17 * TROW, C); put(tx + 20 * TROW, C); put(tx + 21 * TROW, B);
    A = cmul(A, G, Gn); B = cmul(B, G, Gn); C = cmul(C, G, Gn);
    put(tx + 18 * TROW, A); put(tx + 19 * TROW, C); put(tx + 22 * TROW, C); put(tx + 23 * TROW, B);
#pragma unroll
    for (int m = 0; m < 4; ++m) {
      A = cmul(A, G, Gn); B = cmul(B, G, Gn);
      put(tx + (24 + m) * TROW, A); put(tx + (28 + m) * TROW, B);
    }
    const f32x2 F1 = {E8[0], -E8[1]}, F1n = rot(F1);
    const f32x2 F2 = cmul(F1, F1, F1n), F2n = rot(F2);
    const f32x2 F4 = cmul(F2, F2, F2n), F4n = rot(F4);
    f32x2 y0 = {1.f, 0.f}, y1 = F1, y2 = F2, y3 = cmul(F2, F1, F1n);
    put(ty + 0 * TROW, y0); put(ty + 1 * TROW, y1); put(ty + 2 * TROW, y2); put(ty + 3 * TROW, y3);
    y0 = F4;
#pragma unroll
    for (int m = 1; m < 8; ++m) {
      if (m > 1) y0 = cmul(y0, F4, F4n);
      y1 = cmul(y1, F4, F4n); y2 = cmul(y2, F4, F4n); y3 = cmul(y3, F4, F4n);
      put(ty + (4 * m) * TROW, y0); put(ty + (4 * m + 1) * TROW, y1); put(ty + (4 * m + 2) * TROW, y2); put(ty + (4 * m + 3) * TROW, y3);
    }
  }
  wait_vm_bar<0>();
  if (tid == 0) st4_sc1(ws, WS_FX + (xcd * 32 + bi) * FS, tag);
  STAMP(2);
  if (w < 2) {
    const int r = lane & 31, h = lane >> 5;
    const unsigned ta = w * 2 * TSZ + r * TROW + 16 * h;
    f32x16 v;
#pragma unroll
    for (int i = 0; i < 16; ++i) v[i] = 0.f;
#pragma unroll
    for (int s = 0; s < 16; ++s) v = MFMA32(lds_frag(ta + 32 * s), lds_frag(ta + TSZ + 32 * s), v);
    const unsigned ka = S_KR + w * KSZ + (2044 - 8 * r - 4 * h - 1792) * 2;
    *(LAS u32x2*)(ka + 2 * 1792) = u32x2{pk2(v[3], v[2]), pk2(v[1], v[0])};
    *(LAS u32x2*)(ka + 2 * 1536) = u32x2{pk2(v[6], v[5]), pk2(v[4], v[4])};
    *(LAS u32x2*)(ka + 2 * 1280) = u32x2{pk2(v[9], h ? v[9] : v[8]), pk2(v[8], v[8])};
    *(LAS u32x2*)(ka + 2 * 1024) = u32x2{pk2(v[11], h ? v[11] : v[10]), pk2(v[10], v[10])};
#pragma unroll
    for (int a = 0; a < 4; ++a) {
      const float t = v[12 + a];
      auto sw = __builtin_amdgcn_permlane32_swap(__float_as_uint(t), __float_as_uint(t), false, false);
      const unsigned lo = sw[0];
      const float first = (r & 1) ? t : __uint_as_float(lo);
      *(LAS u32x2*)(ka + 2 * (768 - 256 * a)) = u32x2{pk2(t, t), pk2(t, first)};
    }
  } else if (w == 2) {
    if (lane < 60) *(LAS u32x4*)(S_KR + (lane >= 30 ? KSZ - 480 : 0) + 4096 + lane * 16) = u32x4{0u, 0u, 0u, 0u};
    wait_words(ws, WS_FX + (xcd * 32 + (lane & 31)) * FS, tag);
  }
  bar_lgkm();
  STAMP(3);
  {
    const unsigned o = WS_XT + ch * 4096 + n * 32;
    const u32x4 a = ld16_sc1(ws, o), b = ld16_sc1(ws, o + 16);
    const u32x2 c = ld8_sc1(ws, o + 32);
    const unsigned d[10] = {a[0], a[1], a[2], a[3], b[0], b[1], b[2], b[3], c[0], c[1]};
    unsigned e[9];
#pragma unroll
    for (int k = 0; k < 9; ++k) e[k] = __builtin_amdgcn_alignbit(d[k + 1], d[k], 16);
    const unsigned u = chl * USZ + 32 + n * 32;
    *(LAS u32x4*)(u) = a; *(LAS u32x4*)(u + 16) = b;
    *(LAS u32x4*)(u + UCP) = u32x4{e[0], e[1], e[2], e[3]}; *(LAS u32x4*)(u + UCP + 16) = u32x4{e[4], e[5], e[6], e[7]};
    *(LAS u32x4*)(u + 2 * UCP) = u32x4{d[1], d[2], d[3], d[4]}; *(LAS u32x4*)(u + 2 * UCP + 16) = u32x4{d[5], d[6], d[7], d[8]};
    *(LAS u32x4*)(u + 3 * UCP) = u32x4{e[1], e[2], e[3], e[4]}; *(LAS u32x4*)(u + 3 * UCP + 16) = u32x4{e[5], e[6], e[7], e[8]};
    if (n == 0) {
      const u32x4 z = {0u, 0u, 0u, 0u};
      const unsigned u0 = chl * USZ;
      *(LAS u32x4*)(u0) = z; *(LAS u32x4*)(u0 + 16) = z;
      *(LAS u32x4*)(u0 + UCP) = z; *(LAS u32x4*)(u0 + UCP + 16) = u32x4{0u, 0u, 0u, d[0] << 16};
      *(LAS u32x4*)(u0 + 2 * UCP) = z; *(LAS u32x4*)(u0 + 2 * UCP + 16) = u32x4{0u, 0u, 0u, d[0]};
      *(LAS u32x4*)(u0 + 3 * UCP) = z; *(LAS u32x4*)(u0 + 3 * UCP + 16) = u32x4{0u, 0u, d[0] << 16, e[0]};
    }
  }
  bar_lgkm();
  STAMP(4);
  f32x4 acc[8];
#pragma unroll
  for (int c = 0; c < 8; ++c) acc[c] = f32x4{0.f, 0.f, 0.f, 0.f};
  {
    const int sg = (i16 + 1) & 3;
    unsigned ua = chl * USZ + sg * UCP + (i16 + 1 - sg + 8 * g) * 2 + (w & 1) * 256;
    unsigned kb = S_KR + chl * KSZ + (240 - 16 * i16 + 8 * g) * 2 + (w & 1) * 256;
#pragma unroll 1
    for (int rho = 0; rho < 4; ++rho) {
      bf16x8 A[8], G[8];
#pragma unroll
      for (int m = 0; m < 8; ++m) {
        const u32x2 lo = *(LAS u32x2*)(ua + 512 * m), hi = *(LAS u32x2*)(ua + 512 * m + 8);
        A[m] = as_frag(u32x4{lo[0], lo[1], hi[0], hi[1]});
        G[m] = lds_frag(kb + 512 * (7 - m));
      }
#pragma unroll
      for (int c = 0; c < 8; ++c)
#pragma unroll
        for (int m = 0; m <= c; ++m) acc[c] = MFMA16(A[m], G[c - m], acc[c]);
      ua += 64; kb += 64;
    }
  }
  STAMP(5);
  {
    f32x4 keep[4];
    const unsigned ro = S_RED + w * 4096 + lane * 16, ri = S_RED + (w ^ 1) * 4096 + lane * 16;
    if (w & 1) {
#pragma unroll
      for (int k = 0; k < 4; ++k) { *(LAS f32x4*)(ro + k * 1024) = acc[k]; keep[k] = acc[4 + k]; }
    } else {
#pragma unroll
      for (int k = 0; k < 4; ++k) { *(LAS f32x4*)(ro + k * 1024) = acc[4 + k]; keep[k] = acc[k]; }
    }
    bar_lgkm();
    const int c0 = 4 * (w & 1);
    unsigned o2[4][2];
#pragma unroll
    for (int k = 0; k < 4; ++k) {
      const f32x4 t = *(LAS f32x4*)(ri + k * 1024);
      const u32x2 uu = *(LAS u32x2*)(chl * USZ + (256 * (c0 + k) + 16 * i16 + 4 * g + 16) * 2);
      const unsigned u0 = uu[0], u1 = uu[1];
      o2[k][0] = pk2(keep[k][0] + t[0] + dsk * __uint_as_float(u0 << 16), keep[k][1] + t[1] + dsk * __uint_as_float(u0 & 0xFFFF0000u));
      o2[k][1] = pk2(keep[k][2] + t[2] + dsk * __uint_as_float(u1 << 16), keep[k][3] + t[3] + dsk * __uint_as_float(u1 & 0xFFFF0000u));
    }
#pragma unroll
    for (int k = 0; k < 2; ++k) {
      auto s0 = __builtin_amdgcn_permlane16_swap(o2[2 * k][0], o2[2 * k + 1][0], false, false);
      auto s1 = __builtin_amdgcn_permlane16_swap(o2[2 * k][1], o2[2 * k + 1][1], false, false);
      u32x4 v; v[0] = s0[0]; v[1] = s1[0]; v[2] = s0[1]; v[3] = s1[1];
      const int t = 256 * (c0 + 2 * k + (g & 1)) + 16 * i16 + 8 * (g >> 1);
      st16_sc1(ws, WS_Y + ((t >> 6) * 512 + ch) * 128 + (t & 63) * 2, v);
    }
  }
  wait_vm_bar<0>();
  if (tid == 0) st4_sc1(ws, WS_FY + (xcd * 32 + bi) * FS, tag);
  STAMP(6);
  if (sidx == 0 && w == 0) {
#pragma unroll 1
    for (int k = 0; k < 4; ++k) wait_words(ws, WS_FX + (k * 64 + lane) * FS, tag);
    wait_words(ws, WS_L2 + (lane & 7) * 32 * 272 + 4, tag);
    if (lane == 0) st4_sc1(ws, WS_E, epoch + 1u);
  }
  STAMP(7);
  STAMP_FLUSH();
}

__global__ void tiny(char* ws) { const unsigned g0 = load_tag(ws + 1024);
    for (int i = 0; i < 100000; ++i) { if ((int)((unsigned)__builtin_amdgcn_s_memrealtime() - g0) >= 4000) break; __builtin_amdgcn_s_sleep(1); } }
__global__ void __launch_bounds__(256, 2) hh_s4_mix_kernel(Params p) {
  if (blockIdx.x == 0 && threadIdx.x == 0) *(unsigned*)(p.ws + 1024) = (unsigned)__builtin_amdgcn_s_memrealtime();
  if (blockIdx.x < 256) att_block(p, blockIdx.x);
  else ssm_block(p, blockIdx.x - 256);
}

extern "C" void kernel_launch(void* const* d_in, const int* in_sizes, int n_in, void* d_out, int out_size, void* d_ws,
                              size_t ws_size, hipStream_t stream) {
  Params p;
  p.x = (const u16*)d_in[0]; p.wqkv = (const u16*)d_in[1]; p.wphi = (const u16*)d_in[2]; p.wo = (const u16*)d_in[3];
  p.are = (const u16*)d_in[5]; p.aim = (const u16*)d_in[6]; p.bre = (const u16*)d_in[7]; p.bim = (const u16*)d_in[8];
  p.cre = (const u16*)d_in[9]; p.cim = (const u16*)d_in[10]; p.logdt = (const u16*)d_in[11]; p.dskip = (const u16*)d_in[12];
  p.gw = (const u16*)d_in[13];
  p.out = (u16*)d_out;
  p.ws = (char*)d_ws;
  (void)hipFuncSetAttribute((const void*)hh_s4_mix_kernel, hipFuncAttributeMaxDynamicSharedMemorySize, LDS_BYTES);
  hh_s4_mix_kernel<<<512, 256, LDS_BYTES, stream>>>(p);
  tiny<<<1, 64, 0, stream>>>(p.ws);
}
```

```cpp
#include <hip/hip_runtime.h>
#include <hip/hip_bf16.h>
#include <stdint.h>
#pragma clang diagnostic ignored "-Wint-to-pointer-cast"

#define LAS __attribute__((address_space(3)))
#define GAS __attribute__((address_space(1)))
#if defined(__HIP_DEVICE_COMPILE__)
#define NOPK __attribute__((target("no-packed-fp32-ops")))
#else
#define NOPK
#endif
#define DI __device__ __forceinline__ NOPK
typedef __bf16 bf16x8 __attribute__((ext_vector_type(8)));
typedef __bf16 bf16x4 __attribute__((ext_vector_type(4)));
typedef __bf16 bf16x2 __attribute__((ext_vector_type(2)));
typedef float f32x16 __attribute__((ext_vector_type(16)));
typedef float f32x4 __attribute__((ext_vector_type(4)));
typedef float f32x2 __attribute__((ext_vector_type(2)));
typedef unsigned u32x4 __attribute__((ext_vector_type(4)));
typedef unsigned u32x2 __attribute__((ext_vector_type(2)));
typedef unsigned short u16;

#define FUSED 1
#define WARM 0

constexpr unsigned WS_E = 0;
constexpr unsigned FS = 128;
constexpr unsigned WS_FX = 4096;
constexpr unsigned WS_FY = WS_FX + 256 * FS;
constexpr unsigned WS_FO = WS_FY + 256 * FS;
constexpr unsigned WS_L2 = WS_FO + 256 * FS;
constexpr unsigned WS_L1Z = WS_L2 + 8 * 32 * 272;
constexpr unsigned WS_L1K = WS_L1Z + 8 * 32 * 512;
constexpr unsigned WS_XT = WS_L1K + 8 * 32 * 32 * 256;
constexpr unsigned WS_Y = WS_XT + 512 * 4096 + 4096;
constexpr unsigned WS_O = WS_Y + 32 * 512 * 128;
constexpr unsigned WS_ST = WS_O + 128 * 16 * 1024;
constexpr unsigned WS_END = WS_ST + 512 * 4 * 16 * 8;

constexpr unsigned SLOT = 16384;
constexpr unsigned A_XCH = 4 * SLOT;
constexpr unsigned A_WPHI = A_XCH + 8192;
constexpr unsigned A_GW = A_WPHI + 2048;
constexpr unsigned A_XZ = A_GW + 1024;
constexpr unsigned A_GP = A_XZ + 1024;
constexpr unsigned A_DUMMY = A_GP + 1024;
constexpr unsigned A_WO = 0, A_Y = A_XCH;
constexpr unsigned UCP = 4160, USZ = 4 * UCP;
constexpr unsigned S_KR = 2 * USZ, KSZ = 4608;
constexpr unsigned S_XT = S_KR + 2 * KSZ;
constexpr unsigned S_XCH = S_XT + 8192;
constexpr unsigned S_RED = S_XT;
constexpr unsigned LDS_BYTES = 79872;
constexpr int SPIN_CAP = 1 << 17;

struct Params {
  const u16 *x, *wqkv, *wphi, *wo, *are, *aim, *bre, *bim, *cre, *cim, *logdt, *dskip, *gw;
  u16* out;
  char* ws;
};

DI float u2f(unsigned u) { return __builtin_bit_cast(float, u); }
DI unsigned f2u(float f) { return __builtin_bit_cast(unsigned, f); }
DI bool wave_all(bool c) { return __builtin_amdgcn_ballot_w64(!c) == 0ull; }
DI unsigned pk2(float a, float b) { bf16x2 v; v[0] = (__bf16)a; v[1] = (__bf16)b; return __builtin_bit_cast(unsigned, v); }
DI float bf2f(u16 h) { return u2f((unsigned)h << 16); }
DI bf16x8 as_frag(u32x4 v) { return __builtin_bit_cast(bf16x8, v); }
DI bf16x8 pack8(const f32x16& a, int s) {
  u32x4 p;
  p[0] = pk2(a[8 * s + 0], a[8 * s + 1]); p[1] = pk2(a[8 * s + 2], a[8 * s + 3]);
  p[2] = pk2(a[8 * s + 4], a[8 * s + 5]); p[3] = pk2(a[8 * s + 6], a[8 * s + 7]);
  return as_frag(p);
}
DI void dma16(const void* sbase, unsigned voff, unsigned lds) {
  asm volatile("s_mov_b32 m0, %2\n\ts_nop 0\n\tglobal_load_lds_dwordx4 %0, %1" ::"v"(voff), "s"(sbase), "s"(lds) : "memory");
}
DI void dma16_sc1(const void* sbase, unsigned voff, unsigned lds) {
  asm volatile("s_mov_b32 m0, %2\n\ts_nop 0\n\tglobal_load_lds_dwordx4 %0, %1 sc1" ::"v"(voff), "s"(sbase), "s"(lds) : "memory");
}
DI void dma4(const void* sbase, unsigned voff, unsigned lds) {
  asm volatile("s_mov_b32 m0, %2\n\ts_nop 0\n\tglobal_load_lds_dword %0, %1" ::"v"(voff), "s"(sbase), "s"(lds) : "memory");
}
template <int LOFF> DI void dma16i(const void* sbase, unsigned voff, unsigned lbase) {
  asm volatile("s_add_u32 m0, %2, %3\n\ts_nop 0\n\tglobal_load_lds_dwordx4 %0, %1" ::"v"(voff), "s"(sbase), "s"(lbase), "n"(LOFF) : "memory", "scc");
}
#define SB() __builtin_amdgcn_sched_barrier(0)
#define PIN(x) asm volatile("" : "+v"(x))
DI float mul1(float a, float b) { return a * b; }
DI f32x2 pair(const f32x16& a, int i) { return f32x2{a[i], a[i + 1]}; }
DI void swap32(float& a, float& b) {
  auto r = __builtin_amdgcn_permlane32_swap(f2u(a), f2u(b), false, false);
  const unsigned r0 = r[0], r1 = r[1];
  a = u2f(r0); b = u2f(r1);
}
DI void swap16x4(u32x4& a, u32x4& b) {
#pragma unroll
  for (int d = 0; d < 4; ++d) {
    const unsigned x = a[d], y = b[d];
    auto r = __builtin_amdgcn_permlane16_swap(x, y, false, false);
    const unsigned r0 = r[0], r1 = r[1];
    a[d] = r0; b[d] = r1;
  }
}
template <int N> DI void wait_vm_bar() { asm volatile("s_waitcnt vmcnt(%0) lgkmcnt(0)\n\ts_barrier" ::"n"(N) : "memory"); }
DI void bar_lgkm() { asm volatile("s_waitcnt lgkmcnt(0)\n\ts_barrier" ::: "memory"); }
DI bf16x8 lds_frag(unsigned a) { return *(LAS bf16x8*)a; }
DI bf16x4 lds_tr(unsigned a) { return __builtin_amdgcn_ds_read_tr16_b64_v4bf16((LAS bf16x4*)a); }
DI bf16x8 lds_tr2(unsigned a0, unsigned a1) { return __builtin_shufflevector(lds_tr(a0), lds_tr(a1), 0, 1, 2, 3, 4, 5, 6, 7); }
#define MFMA32(a, b, c) __builtin_amdgcn_mfma_f32_32x32x16_bf16((a), (b), (c), 0, 0, 0)
#define MFMA16(a, b, c) __builtin_amdgcn_mfma_f32_16x16x32_bf16((a), (b), (c), 0, 0, 0)
DI float fsum32(float a) {
  auto r = __builtin_amdgcn_permlane32_swap(f2u(a), f2u(a), false, false);
  unsigned r0 = r[0], r1 = r[1];
  return u2f(r0) + u2f(r1);
}
DI float fmax32(float a) {
  auto r = __builtin_amdgcn_permlane32_swap(f2u(a), f2u(a), false, false);
  unsigned r0 = r[0], r1 = r[1];
  return __builtin_fmaxf(u2f(r0), u2f(r1));
}
DI float fmin32(float a) {
  auto r = __builtin_amdgcn_permlane32_swap(f2u(a), f2u(a), false, false);
  unsigned r0 = r[0], r1 = r[1];
  return __builtin_fminf(u2f(r0), u2f(r1));
}
DI float fsum16(float a) {
  auto r = __builtin_amdgcn_permlane16_swap(f2u(a), f2u(a), false, false);
  unsigned r0 = r[0], r1 = r[1];
  return u2f(r0) + u2f(r1);
}
DI float row_sum(float a) {
  a += u2f(__builtin_amdgcn_update_dpp(0u, f2u(a), 0x128, 0xF, 0xF, false));
  a += u2f(__builtin_amdgcn_update_dpp(0u, f2u(a), 0x124, 0xF, 0xF, false));
  a += u2f(__builtin_amdgcn_update_dpp(0u, f2u(a), 0x122, 0xF, 0xF, false));
  a += u2f(__builtin_amdgcn_update_dpp(0u, f2u(a), 0x121, 0xF, 0xF, false));
  return a;
}
DI unsigned load_tag(const char* ws) {
  unsigned e;
  asm volatile("s_load_dword %0, %1, 0x0\n\ts_waitcnt lgkmcnt(0)" : "=s"(e) : "s"(ws) : "memory");
  return e;
}
DI unsigned mk_tag(unsigned e) {
  const unsigned t = ((e + 1u) * 0x1E3779B1u + 0x2FEB352Du) & 0x3FFFFFFFu;
  return (t & 0x3FFFu) | ((t << 1) & 0x3FFF8000u) | ((t << 2) & 0x80000000u);
}
typedef __amdgpu_buffer_rsrc_t rsrc_t;
DI u32x4 ld16_sc1(rsrc_t r, unsigned vo, unsigned so = 0) { return __builtin_amdgcn_raw_buffer_load_b128(r, vo, so, 16); }
DI u32x2 ld8_sc1(rsrc_t r, unsigned vo, unsigned so = 0) { return __builtin_amdgcn_raw_buffer_load_b64(r, vo, so, 16); }
DI unsigned ld4_sc1(rsrc_t r, unsigned vo, unsigned so = 0) { return __builtin_amdgcn_raw_buffer_load_b32(r, vo, so, 16); }
DI void st16_sc1(rsrc_t r, unsigned vo, unsigned so, u32x4 v) { __builtin_amdgcn_raw_buffer_store_b128(v, r, vo + so, 0, 16); }
DI void st8_sc1(rsrc_t r, unsigned vo, unsigned so, u32x2 v) { __builtin_amdgcn_raw_buffer_store_b64(v, r, vo, so, 16); }
DI void st4_sc1(rsrc_t r, unsigned vo, unsigned so, unsigned v) { __builtin_amdgcn_raw_buffer_store_b32(v, r, vo, so, 16); }
DI void wait_words(rsrc_t r, unsigned off, unsigned tag) {
#pragma unroll 1
  for (int sp = 0; sp < SPIN_CAP; ++sp) {
    asm volatile("" ::: "memory");
    unsigned f = ld4_sc1(r, off);
    if (wave_all(f == tag)) break;
    __builtin_amdgcn_s_sleep(2);
  }
  asm volatile("" ::: "memory");
}

#define STAMP(k)
#define STAMP_DECL
#define STAMP_FLUSH()

DI void softmax_exp(const f32x16& P, float (&a)[16], float (&b)[16]) {
  const float L = 1.44269504088896341f;
#define MX3(a, b, c) __builtin_fmaxf(__builtin_fmaxf(a, b), c)
#define MN3(a, b, c) __builtin_fminf(__builtin_fminf(a, b), c)
  float mx0 = MX3(MX3(MX3(MX3(P[0], P[1], P[2]), P[3], P[4]), P[5], P[6]), P[7], P[0]);
  float mx1 = MX3(MX3(MX3(MX3(P[8], P[9], P[10]), P[11], P[12]), P[13], P[14]), P[15], P[8]);
  float mn0 = MN3(MN3(MN3(MN3(P[0], P[1], P[2]), P[3], P[4]), P[5], P[6]), P[7], P[0]);
  float mn1 = MN3(MN3(MN3(MN3(P[8], P[9], P[10]), P[11], P[12]), P[13], P[14]), P[15], P[8]);
  swap32(mx0, mx1); swap32(mn0, mn1);
  mx0 = __builtin_fmaxf(mx0 * L, mx1 * L); mn0 = __builtin_fminf(mn0 * L, mn1 * L);
  mx1 = mx0; mn1 = mn0;
  swap32(mx0, mx1); swap32(mn0, mn1);
#pragma unroll
  for (int j = 0; j < 16; ++j) {
    a[j] = __builtin_amdgcn_exp2f(__builtin_fmaf(P[j], L, j < 8 ? -mx0 : -mx1));
    b[j] = __builtin_amdgcn_exp2f(__builtin_fmaf(P[j], -L, j < 8 ? mn0 : mn1));
  }
}
DI void softmax_rcp(const float (&a)[16], const float (&b)[16], float (&r)[4]) {
  float sa0 = a[0], sa1 = a[8], sb0 = b[0], sb1 = b[8];
#pragma unroll
  for (int j = 1; j < 8; ++j) { sa0 += a[j]; sa1 += a[8 + j]; sb0 += b[j]; sb1 += b[8 + j]; }
  swap32(sa0, sa1); swap32(sb0, sb1);
  sa0 = __builtin_amdgcn_rcpf(sa0 + sa1); sb0 = __builtin_amdgcn_rcpf(sb0 + sb1);
  sa1 = sa0; sb1 = sb0;
  swap32(sa0, sa1); swap32(sb0, sb1);
  r[0] = sa0; r[1] = sb0; r[2] = sa1; r[3] = sb1;
}
DI bf16x8 scale_pack(const float* v, float r) {
  return as_frag(u32x4{pk2(v[0] * r, v[1] * r), pk2(v[2] * r, v[3] * r), pk2(v[4] * r, v[5] * r), pk2(v[6] * r, v[7] * r)});
}

DI void att_block(const Params& p, int b) {
  STAMP_DECL;
  STAMP(0);
  const int tid = threadIdx.x, lane = tid & 63, w = __builtin_amdgcn_readfirstlane(tid >> 6);
  const int xcd = b & 7, bi = b >> 3, ti = 4 * xcd + (bi & 3), q = bi >> 2;
  const int th = w & 1, hp = w >> 1;
  const int rr = lane >> 3, sl = lane & 7;
  const unsigned vx0 = rr * 1024 + ((sl ^ (rr >> 1)) << 4), vx1 = (vx0 ^ 64u) + 8192;
  const unsigned vw0 = rr * 3072 + ((sl ^ (((rr >> 1) & 1) << 2)) << 4), vw1 = vw0 + 24576;
  const char* xs = (const char*)p.x + (unsigned)(64 * ti + 16 * w) * 1024u;
  const char* wsrc = (const char*)p.wqkv + (unsigned)(16 * w) * 3072u + 128 * q;
  const unsigned ldw = w * 2048;
#define XSRC(c) (xs + (((c) + q) & 7) * 128)
#define WSRC(c) (wsrc + (unsigned)(((c) + q) & 7) * 196608u)
#define DX(c, u) dma16i<((c) & 3) * SLOT + (u) * 1024>(XSRC(c), (u) ? vx1 : vx0, ldw)
#define DK(c, u) dma16i<((c) & 3) * SLOT + 8192 + (u) * 1024>(WSRC(c) + 1024, (u) ? vw1 : vw0, ldw)
#define DW(o, c, u) dma16i<(c) * 8192 + (u) * 1024>(WSRC(c) + (o), (u) ? vw1 : vw0, ldw)
  DX(0, 0); DX(0, 1); DK(0, 0); DK(0, 1);
  if (w < 2) dma16((const char*)p.wphi + q * 2048 + w * 1024, lane * 16, A_WPHI + w * 1024);
  else dma16((const char*)p.gw, lane * 16, A_GW);
  DX(1, 0); DX(1, 1); DK(1, 0); DK(1, 1);
  DX(2, 0); DX(2, 1); DK(2, 0); DK(2, 1);
  DX(3, 0); DX(3, 1); DK(3, 0); DK(3, 1);
  SB();
  __builtin_amdgcn_s_setprio(2);
  const int r = lane & 31, h = lane >> 5, blk = (lane >> 4) & 1, i16 = lane & 15, g4 = lane >> 4, qq = i16 >> 2, pp = lane & 3;
  unsigned tag = mk_tag(load_tag(p.ws + WS_E));
  asm volatile("" : "+s"(tag));
  const rsrc_t ws = __builtin_amdgcn_make_buffer_rsrc(p.ws, 0, WS_END, 0x00020000);
  unsigned xch = A_XCH + hp * 4096 + lane * 16, xz = A_XZ + hp * 256 + g4 * 64 + (i16 & 3) * 16;
  unsigned l1k = WS_L1K + ((q * 32 + 16 * hp + g4) * 32 + ti) * 256 + i16 * 16;
  unsigned l1z = WS_L1Z + ((q * 32 + 16 * hp + 4 * i16 + g4) * 32 + ti) * 16;
  unsigned gpa = A_GP + (32 * th + r) * 16 + (2 * hp + h) * 4;
  PIN(xch); PIN(xz); PIN(l1k); PIN(l1z); PIN(gpa);
  u32x4 SEL0, SEL1;
#pragma unroll
  for (int d = 0; d < 4; ++d) {
    const int f0 = ((2 * d) & 3) + 8 * (d >> 1) + 4 * h;
    SEL0[d] = (r == f0 ? 0x3F80u : 0u) | (r == f0 + 1 ? 0x3F800000u : 0u);
    SEL1[d] = (r == f0 + 16 ? 0x3F80u : 0u) | (r == f0 + 17 ? 0x3F800000u : 0u);
  }
  PIN(SEL0); PIN(SEL1);
  unsigned xa[4];
#pragma unroll
  for (int s = 0; s < 4; ++s) xa[s] = (32 * th + r) * 128 + (((2 * (s ^ (2 * hp)) + h) ^ ((r >> 1) & 7)) << 4);
  const unsigned wa_ = (8 * h + qq) * 128 + (((4 * hp + 2 * blk + (pp >> 1)) ^ ((qq >> 1) << 2)) << 4) + 8 * (pp & 1);
  unsigned waA = wa_ + 4096 * hp, waB = wa_ + 4096 - 4096 * hp;
  unsigned gaddr = A_GW + 64 * hp + 16 * h;
  PIN(waA); PIN(waB); PIN(gaddr);
#define WFR(base, s) lds_tr2((base) + ((s) < 2 ? waA : waB) + ((s) & 1) * 2048, (base) + ((s) < 2 ? waA : waB) + ((s) & 1) * 2048 + 512)

  bf16x8 X[32], wk[4];
  u32x4 gv[2];
  f32x16 ak;
  float ga = 0.f, gb = 0.f;
#pragma unroll
  for (int i = 0; i < 16; ++i) ak[i] = 0.f;
#define RDK(c, s)                                                                                     \
  do {                                                                                                \
    X[(4 * (c) + (s)) & 31] = lds_frag(((c) & 3) * SLOT + xa[s]);                                     \
    wk[s] = WFR(((c) & 3) * SLOT + 8192, s);                                                          \
  } while (0)
#define RDG(c, s) gv[s] = *(LAS u32x4*)(gaddr + ((((c) + q) & 7) * 128 + (s) * 32))
#define GATE(c, s)                                                                                    \
  do {                                                                                                \
    const u32x4 xv_ = __builtin_bit_cast(u32x4, X[4 * (c) + (s)]);                                    \
    const unsigned x0 = xv_[0], x1 = xv_[1], x2 = xv_[2], x3 = xv_[3], g0 = gv[s][0], g1 = gv[s][1], g2 = gv[s][2], g3 = gv[s][3]; \
    ga = __builtin_amdgcn_fdot2_f32_bf16(__builtin_bit_cast(bf16x2, x0), __builtin_bit_cast(bf16x2, g0), ga, false); \
    gb = __builtin_amdgcn_fdot2_f32_bf16(__builtin_bit_cast(bf16x2, x1), __builtin_bit_cast(bf16x2, g1), gb, false); \
    ga = __builtin_amdgcn_fdot2_f32_bf16(__builtin_bit_cast(bf16x2, x2), __builtin_bit_cast(bf16x2, g2), ga, false); \
    gb = __builtin_amdgcn_fdot2_f32_bf16(__builtin_bit_cast(bf16x2, x3), __builtin_bit_cast(bf16x2, g3), gb, false); \
    PIN(ga); PIN(gb);                                                                                 \
  } while (0)
  SB();
  wait_vm_bar<13>();
  STAMP(1);
  RDK(0, 0); RDK(0, 1); RDK(0, 2); RDK(0, 3);
#define MK(c, s)                                                                                      \
  do {                                                                                                \
    SB(); ak = MFMA32(wk[s], X[4 * (c) + (s)], ak); PIN(ak); SB();                                    \
    if ((c) < 7) RDK((c) + 1, s);                                                                     \
    SB();                                                                                             \
  } while (0)
#define ITER(c, D0, D1, D2, D3)                                                                       \
  do {                                                                                                \
    wait_vm_bar<8>();                                                                                 \
    D0; RDG(c, 0); RDG(c, 1); MK(c, 0); D1; MK(c, 1); D2; MK(c, 2); D3; MK(c, 3);                     \
    GATE(c, 0); GATE(c, 1); SB();                                                                     \
  } while (0)
  ITER(0, DX(4, 0), DX(4, 1), DK(4, 0), DK(4, 1));
  ITER(1, DX(5, 0), DX(5, 1), DK(5, 0), DK(5, 1));
  ITER(2, DX(6, 0), DX(6, 1), DK(6, 0), DK(6, 1));
  ITER(3, DX(7, 0), DX(7, 1), DK(7, 0), DK(7, 1));
  ITER(4, DW(2048, 0, 0), DW(2048, 0, 1), DW(2048, 1, 0), DW(2048, 1, 1));
  ITER(5, DW(2048, 2, 0), DW(2048, 2, 1), DW(2048, 3, 0), DW(2048, 3, 1));
  ITER(6, DW(2048, 4, 0), DW(2048, 4, 1), DW(2048, 5, 0), DW(2048, 5, 1));
  bf16x8 A1, A2;
  {
    const unsigned a_ = A_WPHI + (2 * hp + blk) * 512 + (4 * h + qq) * 32 + 8 * pp;
    u32x4 wf_;
    const u32x4 z_ = {0u, 0u, 0u, 0u};
    ITER(7, (DW(2048, 6, 0), wf_ = __builtin_bit_cast(u32x4, lds_tr2(a_, a_ + 256))), DW(2048, 6, 1),
         (DW(2048, 7, 0), A1 = as_frag(blk ? z_ : wf_), A2 = as_frag(blk ? wf_ : z_)), DW(2048, 7, 1));
    PIN(A1); PIN(A2);
  }
  STAMP(2);
  f32x16 zero16;
#pragma unroll
  for (int i = 0; i < 16; ++i) zero16[i] = 0.f;
  const f32x4 zero4 = {0.f, 0.f, 0.f, 0.f};
  {
    f32x16 P = MFMA32(A1, pack8(ak, 0), zero16);
    P = MFMA32(A2, pack8(ak, 1), P);
    float ea[16], eb[16], rc[4];
    softmax_exp(P, ea, eb);
#pragma unroll
    for (int j = 0; j < 16; ++j) { PIN(ea[j]); PIN(eb[j]); }
    SB();
    wait_vm_bar<8>();
    STAMP(12);
    f32x16 av = zero16;
    bf16x8 wv[4];
#define VRD(k) wv[(k) & 3] = WFR(((k) >> 2) * 8192, (k) & 3)
#define VMM(k) av = MFMA32(X[k], wv[(k) & 3], av)
    VRD(0); VRD(1); VRD(2);
    SB();
    bf16x8 f[2][2];
    softmax_rcp(ea, eb, rc);
    f[0][0] = scale_pack(ea, rc[0]); f[0][1] = scale_pack(eb, rc[1]);
    f[1][0] = scale_pack(ea + 8, rc[2]); f[1][1] = scale_pack(eb + 8, rc[3]);
#pragma unroll
    for (int k = 0; k < 16; ++k) { if (k + 3 < 16) VRD(k + 3); VMM(k); }
#pragma unroll
    for (int k = 0; k < 16; ++k) {
      __builtin_amdgcn_sched_group_barrier(0x100, 2, 0);
      __builtin_amdgcn_sched_group_barrier(0x008, 1, 0);
      __builtin_amdgcn_sched_group_barrier(0x002, 5, 0);
    }
    PIN(av); PIN(f[0][0]); PIN(f[0][1]); PIN(f[1][0]); PIN(f[1][1]);
    SB();
    wait_vm_bar<0>();
    VRD(16); VRD(17); VRD(18);
    u32x4 t0[2], t1[2], EZ[4];
    f32x16 T;
#define VG(k) SB(); VMM(k); PIN(av); SB(); if ((k) + 3 < 32) VRD((k) + 3)
#define TG(x) SB(); T = x; PIN(T); SB()
#define SW2(e, d) { const unsigned x_ = t0[e][d], y_ = t1[e][d], z_ = t0[e][(d) + 1], u_ = t1[e][(d) + 1];                       \
      auto r_ = __builtin_amdgcn_permlane16_swap(x_, y_, false, false); auto q_ = __builtin_amdgcn_permlane16_swap(z_, u_, false, false); \
      t0[e][d] = r_[0]; t1[e][d] = r_[1]; t0[e][(d) + 1] = q_[0]; t1[e][(d) + 1] = q_[1]; PIN(t0[e]); PIN(t1[e]); }
#define EZB(d) { const unsigned e_ = (i16 == (d)) ? 0x3F803F80u : 0u; EZ[d] = u32x4{e_, e_, e_, e_}; PIN(EZ[d]); }
    TG(MFMA32(f[0][0], as_frag(SEL0), zero16)); DW(0, 0, 0);
    TG(MFMA32(f[0][1], as_frag(SEL1), T)); DW(0, 0, 1);
    VG(16); DW(0, 1, 0);
    VG(17); t0[0] = __builtin_bit_cast(u32x4, pack8(T, 0)); PIN(t0[0]);
    VG(18); t1[0] = __builtin_bit_cast(u32x4, pack8(T, 1)); PIN(t1[0]);
    TG(MFMA32(f[1][0], as_frag(SEL0), zero16)); SW2(0, 0);
    TG(MFMA32(f[1][1], as_frag(SEL1), T)); SW2(0, 2);
    VG(19); DW(0, 1, 1);
    VG(20); DW(0, 2, 0);
    VG(21); t0[1] = __builtin_bit_cast(u32x4, pack8(T, 0)); PIN(t0[1]);
    VG(22); t1[1] = __builtin_bit_cast(u32x4, pack8(T, 1)); PIN(t1[1]);
    VG(23); SW2(1, 0);
    VG(24); SW2(1, 2);
    VG(25); DW(0, 2, 1);
    VG(26); EZB(0);
    VG(27); EZB(1);
    VG(28); DW(0, 3, 0); EZB(2);
    VG(29); DW(0, 3, 1); EZB(3);
    VG(30);
    VG(31);
    SB();
    f32x4 Z = MFMA16(as_frag(t0[0]), as_frag(EZ[0]), zero4);
    Z = MFMA16(as_frag(t1[0]), as_frag(EZ[1]), Z);
    Z = MFMA16(as_frag(t0[1]), as_frag(EZ[2]), Z);
    Z = MFMA16(as_frag(t1[1]), as_frag(EZ[3]), Z);
    PIN(Z);
    u32x4 vv[2] = {__builtin_bit_cast(u32x4, pack8(av, 0)), __builtin_bit_cast(u32x4, pack8(av, 1))};
    swap16x4(vv[0], vv[1]);
    f32x4 R[4];
    R[0] = MFMA16(as_frag(t0[0]), as_frag(vv[0]), zero4); R[1] = MFMA16(as_frag(t1[0]), as_frag(vv[0]), zero4);
    R[2] = MFMA16(as_frag(t0[1]), as_frag(vv[1]), zero4); R[3] = MFMA16(as_frag(t1[1]), as_frag(vv[1]), zero4);
    *(LAS float*)gpa = ga + gb;
    if (th) {
      if (i16 < 4) *(LAS f32x4*)xz = Z;
#pragma unroll
      for (int k = 0; k < 4; ++k) *(LAS f32x4*)(xch + k * 1024) = R[k];
      bar_lgkm();
    } else {
      bar_lgkm();
      f32x4 o[4];
#pragma unroll
      for (int k = 0; k < 4; ++k) o[k] = *(LAS f32x4*)(xch + k * 1024);
      const f32x4 oz = *(LAS f32x4*)xz;
#pragma unroll
      for (int k = 0; k < 4; ++k) {
        const f32x4 t = R[k] + o[k];
        st16_sc1(ws, l1k, k * 4 * 32 * 256, u32x4{pk2(t[0], t[1]), tag, pk2(t[2], t[3]), tag});
      }
      Z += oz;
      if (i16 < 4) st16_sc1(ws, l1z, 0, u32x4{pk2(Z[0], Z[1]), tag, pk2(Z[2], Z[3]), tag});
    }
  }
  SB();
  STAMP(3);
  const unsigned vwo = rr * 1024 + ((sl ^ (((rr >> 1) & 3) << 1)) << 4);
  const char* wos = (const char*)p.wo + 128 * q + (unsigned)w * 8192u;
#define DO(i) dma16i<A_WO + (i) * 4096>(wos + (i) * 32768, vwo, (unsigned)w * 1024u)
  bf16x8 pq[2][2];
  f32x16 PQ;
  {
    f32x16 aq = zero16;
    bf16x8 wq[8];
#define QRD(k) wq[(k) & 7] = WFR(((k) >> 2) * 8192, (k) & 3)
#define QG(k, k1) SB(); aq = MFMA32(wq[(k) & 7], X[k], aq); PIN(aq); SB(); if ((k) + 4 < (k1)) QRD((k) + 4)
#define DQ(c, u, j) dma16i<(c) * 8192 + (u) * 1024 + (j) * 2048 - 2048>(WSRC(c) + (j) * 49152 - 49152, (u) ? vw1 : vw0, ldw)
    if (th) {
      wait_vm_bar<0>();
      QRD(0); QRD(1); QRD(2); QRD(3);
      DQ(4, 0, 0); QG(0, 16); DQ(4, 1, 0); QG(1, 16); DQ(4, 0, 1); QG(2, 16); DQ(4, 1, 1); QG(3, 16);
      DQ(5, 0, 0); QG(4, 16); DQ(5, 1, 0); QG(5, 16); DQ(5, 0, 1); QG(6, 16); DQ(5, 1, 1); QG(7, 16);
      DQ(6, 0, 0); QG(8, 16); DQ(6, 1, 0); QG(9, 16); DQ(6, 0, 1); QG(10, 16); DQ(6, 1, 1); QG(11, 16);
      DQ(7, 0, 0); QG(12, 16); DQ(7, 1, 0); QG(13, 16); DQ(7, 0, 1); QG(14, 16); DQ(7, 1, 1); QG(15, 16);
      SB();
      STAMP(4);
      {
      const unsigned s01 = i16 == 0 ? 0x00003F80u : i16 == 1 ? 0x3F800000u : 0u, s23 = i16 == 2 ? 0x00003F80u : i16 == 3 ? 0x3F800000u : 0u;
      const bf16x8 SUM = as_frag(u32x4{s01, 0u, s23, 0u});
      const unsigned st = hp ? 16u : 256u;
      const unsigned o = g4 * 8 * st + (hp ? 0 : i16 * 16), so = hp ? WS_L1Z + (q * 32 + ti) * 512 : WS_L1K + (q * 32 + ti) * 8192;
      u32x4 g[8];
#pragma unroll 1
      for (int sp = 0; sp < SPIN_CAP; ++sp) {
        asm volatile("" ::: "memory");
        unsigned bad = 0;
#pragma unroll
        for (int i = 0; i < 8; ++i) g[i] = ld16_sc1(ws, o, so + i * st);
#pragma unroll
        for (int i = 0; i < 8; ++i) bad |= (g[i][1] ^ tag) | (g[i][3] ^ tag);
        if (wave_all(bad == 0)) break;
        __builtin_amdgcn_s_sleep(1);
      }
      f32x4 t = zero4;
#pragma unroll
      for (int i = 0; i < 8; ++i) t = MFMA16(SUM, as_frag(g[i]), t);
      if (lane < (hp ? 1 : 16)) st16_sc1(ws, i16 * 16, WS_L2 + (q * 32 + ti) * 272 + hp * 256, u32x4{pk2(t[0], t[1]), tag, pk2(t[2], t[3]), tag});
      }
      SB();
      STAMP(5);
      wait_vm_bar<1>();
    } else {
      wait_vm_bar<5>();
      QRD(0); QRD(1); QRD(2); QRD(3);
#pragma unroll
      for (int k = 0; k < 16; ++k) { QG(k, 16); }
      SB();
      bar_lgkm();
    }
    QRD(16); QRD(17); QRD(18); QRD(19);
    DO(0); QG(16, 32); QG(17, 32); DO(1); QG(18, 32); QG(19, 32); DO(2); QG(20, 32); QG(21, 32); DO(3); QG(22, 32); QG(23, 32);
    DO(4); QG(24, 32); QG(25, 32); DO(5); QG(26, 32); QG(27, 32); DO(6); QG(28, 32); QG(29, 32); DO(7); QG(30, 32); QG(31, 32);
    SB();

    PQ = MFMA32(A1, pack8(aq, 0), zero16);
    PQ = MFMA32(A2, pack8(aq, 1), PQ);
    PIN(PQ);
  }
  SB();
  SB();
  {
    float ea[16], eb[16], rc[4];
    softmax_exp(PQ, ea, eb);
    softmax_rcp(ea, eb, rc);
    pq[0][0] = scale_pack(ea, rc[0]); pq[0][1] = scale_pack(eb, rc[1]);
    pq[1][0] = scale_pack(ea + 8, rc[2]); pq[1][1] = scale_pack(eb + 8, rc[3]);
    PIN(pq[0][0]); PIN(pq[0][1]); PIN(pq[1][0]); PIN(pq[1][1]);
  }
  SB();
  STAMP(6);
  {
    unsigned o = h * 272 + (r < 16 ? r : 16) * 16, oo = blk * 16384 + (i16 + 16 * h) * 16;
    PIN(o); PIN(oo);
    const unsigned so = WS_L2 + (q * 32 + hp * 16) * 272, soo = WS_O + ((4 * ti + 2 * th) * 16 + 2 * q + hp) * 1024;
    u32x4 g[8];
#pragma unroll 1
    for (int sp = 0; sp < SPIN_CAP; ++sp) {
      asm volatile("" ::: "memory");
      unsigned bad = 0;
#pragma unroll
      for (int i = 0; i < 8; ++i) g[i] = ld16_sc1(ws, o + i * 544, so);
#pragma unroll
      for (int i = 0; i < 8; ++i) bad |= (g[i][1] ^ tag) | (g[i][3] ^ tag);
      if (wave_all(bad == 0)) break;
      __builtin_amdgcn_s_sleep(1);
    }
    STAMP(8);
    f32x16 Dn[2];
#pragma unroll
    for (int e = 0; e < 2; ++e) {
      const u32x4 f0 = {g[4 * e][0], g[4 * e][2], g[4 * e + 1][0], g[4 * e + 1][2]};
      const u32x4 f1 = {g[4 * e + 2][0], g[4 * e + 2][2], g[4 * e + 3][0], g[4 * e + 3][2]};
      Dn[e] = MFMA32(as_frag(f0), pq[e][0], zero16);
      Dn[e] = MFMA32(as_frag(f1), pq[e][1], Dn[e]);
    }
#pragma unroll
    for (int e = 0; e < 2; ++e) {
      const float rd = __builtin_amdgcn_rcpf(Dn[e][8] + 1e-6f);
      u32x4 ov;
#pragma unroll
      for (int j = 0; j < 4; ++j) ov[j] = pk2(mul1(Dn[e][2 * j], rd), mul1(Dn[e][2 * j + 1], rd));
      st16_sc1(ws, oo + e * 512, soo, ov);
    }
  }
  wait_vm_bar<0>();
  if (tid == 0) st4_sc1(ws, 0, WS_FO + (ti * 8 + q) * FS, tag);
  DO(8); DO(9); DO(10); DO(11); DO(12); DO(13); DO(14); DO(15);
  STAMP(9);
  {
    const int key = (2 * (g4 & 1) + (qq >> 1)) & 3;
    unsigned abase[4];
#pragma unroll
    for (int ct = 0; ct < 4; ++ct)
      abase[ct] = A_WO + (16 * (g4 >> 1) + 4 * (g4 & 1) + qq) * 128 + (((2 * ct + (pp >> 1)) ^ (key << 1)) << 4) + 8 * (pp & 1);
    bf16x8 A[8][4];
#pragma unroll
    for (int k = 0; k < 8; ++k)
#pragma unroll
      for (int ct = 0; ct < 4; ++ct) { A[k][ct] = lds_tr2(abase[ct] + k * 4096, abase[ct] + k * 4096 + 1024); }
    const f32x4 gp = *(LAS f32x4*)(A_GP + (16 * w + i16) * 16);
    float gate = __builtin_amdgcn_rcpf(1.f + __builtin_amdgcn_exp2f(-1.44269504088896341f * ((gp[0] + gp[1]) + (gp[2] + gp[3]))));
    PIN(gate);
#pragma unroll
    for (int k = 0; k < 8; ++k)
#pragma unroll
      for (int ct = 0; ct < 4; ++ct) PIN(A[k][ct]);
    const char* ysrc = p.ws + WS_Y + (unsigned)(ti * 512 + 64 * q + 16 * w) * 128u;
    const unsigned yvo = rr * 128 + ((sl ^ (((rr >> 1) & 3) << 1)) << 4);
    const unsigned obase = WS_O + (unsigned)(4 * ti + w) * 16384u;
    SB();
    const unsigned fo = lane < 32 ? WS_FY + (q * 32 + lane) * FS : WS_FO + (ti * 8 + (lane & 7)) * FS;
    unsigned long long miss;
#pragma unroll 1
    for (int sp = 0; sp < SPIN_CAP; ++sp) {
      asm volatile("" ::: "memory");
      miss = __builtin_amdgcn_ballot_w64(ld4_sc1(ws, fo) != tag);
      if ((miss >> 32) == 0) break;
      __builtin_amdgcn_s_sleep(1);
    }
    asm volatile("" ::: "memory");
    STAMP(10);
    if (miss == 0) { dma16_sc1(ysrc, yvo, A_Y + w * 2048); dma16_sc1(ysrc + 1024, yvo, A_Y + w * 2048 + 1024); }
    u32x4 of[16];
#pragma unroll
    for (int k = 0; k < 16; ++k) of[k] = ld16_sc1(ws, lane * 16 + (k & 3) * 1024, obase + (k >> 2) * 4096);
    SB();
    asm volatile("s_barrier" ::: "memory");
    f32x4 acc[4] = {zero4, zero4, zero4, zero4};
#pragma unroll
    for (int k = 0; k < 16; ++k) {
#pragma unroll
      for (int ct = 0; ct < 4; ++ct) {
        acc[ct] = MFMA16(A[k & 7][ct], as_frag(of[k]), acc[ct]);
        SB();
        if (k < 8) A[k][ct] = lds_tr2(abase[ct] + (k + 8) * 4096, abase[ct] + (k + 8) * 4096 + 1024);
        SB();
      }
    }
    if (miss != 0) {
      wait_words(ws, fo, tag);
      dma16_sc1(ysrc, yvo, A_Y + w * 2048); dma16_sc1(ysrc + 1024, yvo, A_Y + w * 2048 + 1024);
    }
    STAMP(7);
    wait_vm_bar<0>();
    const int ykey = (2 * g4 + (qq >> 1)) & 3;
    const unsigned ybase = A_Y + (4 * g4 + qq) * 128 + (((2 * w + (pp >> 1)) ^ (ykey << 1)) << 4) + 8 * (pp & 1);
    unsigned o2[4][2];
#pragma unroll
    for (int ct = 0; ct < 4; ++ct) {
      const bf16x4 yb = lds_tr(ybase + ct * 2048);
      float t[4];
#pragma unroll
      for (int j = 0; j < 4; ++j) { const float bv = (float)yb[j]; t[j] = bv + gate * (acc[ct][j] - bv); }
      o2[ct][0] = pk2(t[0], t[1]); o2[ct][1] = pk2(t[2], t[3]);
    }
    char* orow = (char*)p.out + (unsigned)(64 * ti + 16 * w + i16) * 1024u + 128 * q + 32 * (g4 & 1) + 16 * (g4 >> 1);
#pragma unroll
    for (int c2 = 0; c2 < 2; ++c2) {
      auto s0 = __builtin_amdgcn_permlane16_swap(o2[2 * c2][0], o2[2 * c2 + 1][0], false, false);
      auto s1 = __builtin_amdgcn_permlane16_swap(o2[2 * c2][1], o2[2 * c2 + 1][1], false, false);
      u32x4 v; v[0] = s0[0]; v[1] = s1[0]; v[2] = s0[1]; v[3] = s1[1];
      *(GAS u32x4*)(orow + 64 * c2) = v;
    }
  }
  STAMP(11);
  STAMP_FLUSH();
}

DI f32x2 cexp_l(float lr2, float li2, float l) {
  const float m = __builtin_amdgcn_exp2f(lr2 * l), ph = __builtin_amdgcn_fractf(li2 * l);
  return f32x2{m * __builtin_amdgcn_cosf(ph), m * __builtin_amdgcn_sinf(ph)};
}
#define BC "row_newbcast:%c5 row_mask:0xf bank_mask:0xf bound_ctrl:1"
template <int J> DI float bcmul(float x, float y) {
  float r;
  asm("v_mul_f32_dpp %0, %1, %2 row_newbcast:%3 row_mask:0xf bank_mask:0xf bound_ctrl:1" : "=v"(r) : "v"(x), "v"(y), "n"(J));
  return r;
}
template <int J> DI void bccmul(float ar, float ai, float br, float bi, float& re, float& im) {
  asm("v_mul_f32_dpp %0, %4, %2 row_newbcast:%6 row_mask:0xf bank_mask:0xf bound_ctrl:1\n\t"
      "v_mul_f32_dpp %1, %5, %2 row_newbcast:%6 row_mask:0xf bank_mask:0xf bound_ctrl:1\n\t"
      "v_fmac_f32_dpp %0, -%5, %3 row_newbcast:%6 row_mask:0xf bank_mask:0xf bound_ctrl:1\n\t"
      "v_fmac_f32_dpp %1, %4, %3 row_newbcast:%6 row_mask:0xf bank_mask:0xf bound_ctrl:1"
      : "=&v"(re), "=&v"(im) : "v"(ar), "v"(ai), "v"(br), "v"(bi), "n"(J));
}
struct Mode { float lr2, li2, wr, wi, gr, gi, fr, fi; };
template <int J> DI void kslot(const Mode& m, float rho, float r8, float r8n, unsigned& a0, unsigned& a1, unsigned& b0, unsigned& b1) {
  const float pa = __builtin_amdgcn_fractf(bcmul<J>(m.li2, rho)), pb = bcmul<J>(m.li2, r8n);
  const float ma = __builtin_amdgcn_exp2f(bcmul<J>(m.lr2, rho)), mb = __builtin_amdgcn_exp2f(bcmul<J>(m.lr2, r8));
  const float er = ma * __builtin_amdgcn_cosf(pa), ei = ma * __builtin_amdgcn_sinf(pa);
  const float yr = mb * __builtin_amdgcn_cosf(pb), yi = mb * __builtin_amdgcn_sinf(pb);
  float xr, xi, ur, ui, zr, zi;
  bccmul<J>(er, ei, m.wr, m.wi, xr, xi);
  bccmul<J>(yr, yi, m.fr, m.fi, zr, zi);
  bccmul<J>(xr, xi, m.gr, m.gi, ur, ui);
  a0 = pk2(xr, xi); a1 = pk2(ur, ui); b0 = pk2(yr, yi); b1 = pk2(zr, zi);
}
DI void split_pair(unsigned v, int h, float& lo_arr, float& hi_arr) {
  auto r = __builtin_amdgcn_permlane32_swap(v, v, false, false);
  const unsigned r0 = r[0], r1 = r[1], sh = h ? 0u : 16u;
  lo_arr = u2f((r0 << sh) & 0xFFFF0000u); hi_arr = u2f((r1 << sh) & 0xFFFF0000u);
}
DI float half_bf16(unsigned v, int idx) { return u2f((idx & 1) ? (v & 0xFFFF0000u) : (v << 16)); }

DI void ssm_block(const Params& p, int sidx) {
  STAMP_DECL;
  STAMP(0);
  const int tid = threadIdx.x, lane = tid & 63, w = __builtin_amdgcn_readfirstlane(tid >> 6);
  const int xcd = sidx & 7, bi = sidx >> 3;
  const int i16 = lane & 15, g = lane >> 4, qq = i16 >> 2, pp = lane & 3;
  const int chl = w >> 1, kh = w & 1, ch = 64 * xcd + 2 * bi + chl;
  __builtin_amdgcn_s_setprio(3);
  {
    const char* xs = (const char*)p.x + (unsigned)(64 * bi + 16 * w) * 1024u + 128 * xcd;
    const unsigned vo = (lane >> 3) * 1024 + (lane & 7) * 16;
    dma16(xs, vo, S_XT + w * 2048);
    dma16(xs + 8192, vo, S_XT + w * 2048 + 1024);
  }
  const int n = tid & 127, hh = lane >> 5;
  const unsigned po = (ch * 128 + 64 * kh) * 2 + (lane & 31) * 4;
  const unsigned pA = *(const GAS unsigned*)((const char*)(hh ? p.aim : p.are) + po);
  const unsigned pB = *(const GAS unsigned*)((const char*)(hh ? p.bim : p.bre) + po);
  const unsigned pC = *(const GAS unsigned*)((const char*)(hh ? p.cim : p.cre) + po);
  SB();
  __builtin_amdgcn_s_setprio(0);
  unsigned epoch, sdt, sds;
  asm volatile("s_load_dword %0, %3, 0x0\n\ts_load_dword %1, %4, 0x0\n\ts_load_dword %2, %5, 0x0\n\ts_waitcnt lgkmcnt(0)"
               : "=&s"(sdt), "=&s"(sds), "=&s"(epoch)
               : "s"((const char*)p.logdt + (ch >> 1) * 4), "s"((const char*)p.dskip + (ch >> 1) * 4), "s"(p.ws + WS_E) : "memory");
  const float ldt = half_bf16(sdt, ch), dsk = half_bf16(sds, ch);
  unsigned tag = mk_tag(epoch);
  asm volatile("" : "+s"(tag));
  const rsrc_t ws = __builtin_amdgcn_make_buffer_rsrc(p.ws, 0, WS_END, 0x00020000);
  const int r2_ = i16 == 11 ? 8 : 480 + 4 * i16, r3_ = 1024 + 8 * (i16 & 1) + 256 * (i16 & 2);
  float rho = (float)(i16 < 8 ? i16 : i16 < 12 ? r2_ : r3_);
  float r8 = 8.f * i16, r8n = -r8;
  PIN(rho); PIN(r8); PIN(r8n);
  const float dt = (float)(__bf16)__builtin_expf(ldt);
  {
    unsigned pa_ = pA, pb_ = pB, pc_ = pC;
    PIN(pa_); PIN(pb_); PIN(pc_);
  }
  STAMP(1);
  Mode md;
  {
    float are, aim, bre, bim, cre, cim;
    split_pair(pA, hh, are, aim); split_pair(pB, hh, bre, bim); split_pair(pC, hh, cre, cim);
    md.lr2 = dt * are * 1.44269504088896341f;
    md.li2 = dt * aim * 0.15915494309189535f;
    md.li2 -= __builtin_rintf(md.li2);
    const float dbr = dt * bre, dbi = dt * bim;
    md.wr = cre * dbr - cim * dbi; md.wi = cre * dbi + cim * dbr;
    const f32x2 G = cexp_l(md.lr2, md.li2, 256.f), F = cexp_l(md.lr2, -md.li2, 128.f);
    md.gr = G[0]; md.gi = G[1]; md.fr = F[0]; md.fi = F[1];
    asm volatile("s_nop 1" : "+v"(md.lr2), "+v"(md.li2), "+v"(md.wr), "+v"(md.wi), "+v"(md.gr), "+v"(md.gi), "+v"(md.fr), "+v"(md.fi));
  }
  const f32x4 zero4 = {0.f, 0.f, 0.f, 0.f};
  f32x4 D[2][2] = {{zero4, zero4}, {zero4, zero4}};
#define KSTEP(s)                                                                                      \
  do {                                                                                                \
    unsigned a0[4], a1[4], b0[4], b1[4];                                                              \
    kslot<4 * (s) + 0>(md, rho, r8, r8n, a0[0], a1[0], b0[0], b1[0]);                        \
    kslot<4 * (s) + 1>(md, rho, r8, r8n, a0[1], a1[1], b0[1], b1[1]);                        \
    kslot<4 * (s) + 2>(md, rho, r8, r8n, a0[2], a1[2], b0[2], b1[2]);                        \
    kslot<4 * (s) + 3>(md, rho, r8, r8n, a0[3], a1[3], b0[3], b1[3]);                        \
    const bf16x8 A0 = as_frag(u32x4{a0[0], a0[1], a0[2], a0[3]}), A1 = as_frag(u32x4{a1[0], a1[1], a1[2], a1[3]}); \
    const bf16x8 B0 = as_frag(u32x4{b0[0], b0[1], b0[2], b0[3]}), B1 = as_frag(u32x4{b1[0], b1[1], b1[2], b1[3]}); \
    D[0][0] = MFMA16(A0, B0, D[0][0]); D[0][1] = MFMA16(A0, B1, D[0][1]);                             \
    D[1][0] = MFMA16(A1, B0, D[1][0]); D[1][1] = MFMA16(A1, B1, D[1][1]);                             \
    PIN(D[0][0]); PIN(D[0][1]); PIN(D[1][0]); PIN(D[1][1]);                                           \
  } while (0)
  KSTEP(0);
  wait_vm_bar<WARM ? 2 : 0>();
  {
    const unsigned a = S_XT + (8 * g + qq) * 128 + (16 * w + 4 * pp) * 2;
    const unsigned vo = i16 * 4096 + g * 16, so = WS_XT + (64 * xcd + 16 * w) * 4096 + 128 * bi;
    st16_sc1(ws, vo, so, __builtin_bit_cast(u32x4, lds_tr2(a, a + 512)));
    st16_sc1(ws, vo + 64, so, __builtin_bit_cast(u32x4, lds_tr2(a + 4096, a + 4096 + 512)));
  }
  SB();
  KSTEP(1);
  KSTEP(2);
  wait_vm_bar<0>();
  if (tid == 0) st4_sc1(ws, 0, WS_FX + (xcd * 32 + bi) * FS, tag);
  STAMP(8);
  KSTEP(3);
  STAMP(2);
  *(LAS f32x4*)(S_XCH + w * 2048 + lane * 16) = kh ? D[0][0] : D[0][1];
  *(LAS f32x4*)(S_XCH + w * 2048 + 1024 + lane * 16) = kh ? D[1][0] : D[1][1];
  wait_words(ws, WS_FX + (xcd * 32 + (lane & 31)) * FS, tag);
  asm volatile("" ::: "memory");
  const u32x4 ua_ = ld16_sc1(ws, n * 32, WS_XT + ch * 4096), ub_ = ld16_sc1(ws, n * 32 + 16, WS_XT + ch * 4096);
  const u32x2 uc_ = ld8_sc1(ws, n * 32 + 32, WS_XT + ch * 4096);
  STAMP(9);
  bar_lgkm();
  {
    const f32x4 k0 = (kh ? D[0][1] : D[0][0]) + *(LAS f32x4*)(S_XCH + (w ^ 1) * 2048 + lane * 16);
    const f32x4 k1 = (kh ? D[1][1] : D[1][0]) + *(LAS f32x4*)(S_XCH + (w ^ 1) * 2048 + 1024 + lane * 16);
    float t0 = k1[0], t1 = t0, u0 = k1[3], u1 = u0;
    { auto r_ = __builtin_amdgcn_permlane16_swap(f2u(t0), f2u(t1), false, false); const unsigned x_ = r_[1]; t1 = u2f(x_); }
    swap32(u0, u1); u0 = u1;
    { auto r_ = __builtin_amdgcn_permlane16_swap(f2u(u0), f2u(u1), false, false); const unsigned x_ = r_[0]; u0 = u2f(x_); }
    const unsigned ka = S_KR + chl * KSZ + (248 - 128 * kh - 8 * i16) * 2;
    if (g < 2) {
      const float nx = g ? u0 : t1;
      *(LAS u32x2*)(ka + 2 * 1792 + 8 - 8 * g) = u32x2{pk2(k0[3], k0[2]), pk2(k0[1], k0[0])};
      *(LAS u32x2*)(ka + 2 * 1536 + 8 - 8 * g) = u32x2{pk2(nx, k1[2]), pk2(k1[0], k1[0])};
    } else if (g == 2) {
      *(LAS u32x4*)(ka + 2 * 1280) = u32x4{pk2(k0[2], k0[2]), pk2(k0[1], k0[1]), pk2(k0[1], k0[0]), pk2(k0[0], k0[0])};
      *(LAS u32x4*)(ka + 2 * 1024) = u32x4{pk2(k1[2], k1[2]), pk2(k1[1], k1[1]), pk2(k1[1], k1[0]), pk2(k1[0], k1[0])};
    } else {
      const bool odd = i16 & 1;
      const unsigned c0 = pk2(k0[0], k0[0]), c1 = pk2(k1[0], k1[0]), c2 = pk2(k0[2], k0[2]), c3 = pk2(k1[2], k1[2]);
      *(LAS u32x4*)(ka + 2 * 768) = u32x4{pk2(k0[1], k0[1]), pk2(k0[1], odd ? k0[1] : k0[0]), c0, c0};
      *(LAS u32x4*)(ka + 2 * 512) = u32x4{pk2(k1[1], k1[1]), pk2(k1[1], odd ? k1[1] : k1[0]), c1, c1};
      *(LAS u32x4*)(ka + 2 * 256) = u32x4{pk2(k0[3], k0[3]), pk2(k0[3], odd ? k0[3] : k0[2]), c2, c2};
      *(LAS u32x4*)(ka) = u32x4{pk2(k1[3], k1[3]), pk2(k1[3], odd ? k1[3] : k1[2]), c3, c3};
    }
    if (kh == 0 && lane < 30) *(LAS u32x4*)(S_KR + chl * KSZ + 4096 + lane * 16) = u32x4{0u, 0u, 0u, 0u};
  }
  STAMP(3);
  {
    const unsigned d[10] = {ua_[0], ua_[1], ua_[2], ua_[3], ub_[0], ub_[1], ub_[2], ub_[3], uc_[0], uc_[1]};
    unsigned e[9];
#pragma unroll
    for (int k = 0; k < 9; ++k) e[k] = __builtin_amdgcn_alignbit(d[k + 1], d[k], 16);
    const unsigned u = chl * USZ + 32 + n * 32;
    *(LAS u32x4*)(u) = ua_; *(LAS u32x4*)(u + 16) = ub_;
    *(LAS u32x4*)(u + UCP) = u32x4{e[0], e[1], e[2], e[3]}; *(LAS u32x4*)(u + UCP + 16) = u32x4{e[4], e[5], e[6], e[7]};
    *(LAS u32x4*)(u + 2 * UCP) = u32x4{d[1], d[2], d[3], d[4]}; *(LAS u32x4*)(u + 2 * UCP + 16) = u32x4{d[5], d[6], d[7], d[8]};
    *(LAS u32x4*)(u + 3 * UCP) = u32x4{e[1], e[2], e[3], e[4]}; *(LAS u32x4*)(u + 3 * UCP + 16) = u32x4{e[5], e[6], e[7], e[8]};
    if (n == 0) {
      const u32x4 z = {0u, 0u, 0u, 0u};
      const unsigned u0 = chl * USZ;
      *(LAS u32x4*)(u0) = z; *(LAS u32x4*)(u0 + 16) = z;
      *(LAS u32x4*)(u0 + UCP) = z; *(LAS u32x4*)(u0 + UCP + 16) = u32x4{0u, 0u, 0u, d[0] << 16};
      *(LAS u32x4*)(u0 + 2 * UCP) = z; *(LAS u32x4*)(u0 + 2 * UCP + 16) = u32x4{0u, 0u, 0u, d[0]};
      *(LAS u32x4*)(u0 + 3 * UCP) = z; *(LAS u32x4*)(u0 + 3 * UCP + 16) = u32x4{0u, 0u, d[0] << 16, e[0]};
    }
  }
  bar_lgkm();
  STAMP(4);
  f32x4 acc[8];
#pragma unroll
  for (int c = 0; c < 8; ++c) acc[c] = f32x4{0.f, 0.f, 0.f, 0.f};
  {
    const int sg = (i16 + 1) & 3;
    const unsigned ua = chl * USZ + sg * UCP + (i16 + 1 - sg + 8 * g) * 2 + (w & 1) * 256;
    const unsigned kb = S_KR + chl * KSZ + (240 - 16 * i16 + 8 * g) * 2 + (w & 1) * 256;
    unsigned ub = ua + 8;
    PIN(ub);
    u32x2 Al[2][8], Ah[2][8];
    bf16x8 G[2][8];
#define CRD(bf, rho, i)                                                                               \
  do {                                                                                                \
    if ((i) % 3 == 0) Al[bf][(i) / 3] = *(LAS u32x2*)(ua + 64 * (rho) + 512 * ((i) / 3));             \
    else if ((i) % 3 == 1) Ah[bf][(i) / 3] = *(LAS u32x2*)(ub + 64 * (rho) + 512 * ((i) / 3));        \
    else G[bf][(i) / 3] = lds_frag(kb + 64 * (rho) + 512 * (7 - (i) / 3));                            \
  } while (0)
#pragma unroll
    for (int i = 0; i < 24; ++i) { CRD(0, 0, i); SB(); }
#pragma unroll
    for (int rho = 0; rho < 4; ++rho) {
      int cnt = 0;
#pragma unroll
      for (int dsum = 0; dsum < 8; ++dsum) {
#pragma unroll
        for (int k = 0; k <= 2 * dsum; ++k) {
          const int m = k <= dsum ? k : dsum, nn = k <= dsum ? dsum : 2 * dsum - k;
          if (m + nn < 8) {
            SB();
            const u32x4 af = {Al[rho & 1][m][0], Al[rho & 1][m][1], Ah[rho & 1][m][0], Ah[rho & 1][m][1]};
            acc[m + nn] = MFMA16(as_frag(af), G[rho & 1][nn], acc[m + nn]);
            PIN(acc[m + nn]);
            SB();
            if (rho < 3 && cnt % 3 != 2 && cnt - cnt / 3 < 24) CRD((rho + 1) & 1, rho + 1, cnt - cnt / 3);
            ++cnt;
          }
        }
      }
    }
    SB();
  }
  STAMP(5);
  {
    f32x4 keep[4];
    const unsigned ro = S_RED + w * 4096 + lane * 16, ri = S_RED + (w ^ 1) * 4096 + lane * 16;
    if (w & 1) {
#pragma unroll
      for (int k = 0; k < 4; ++k) { *(LAS f32x4*)(ro + k * 1024) = acc[k]; keep[k] = acc[4 + k]; }
    } else {
#pragma unroll
      for (int k = 0; k < 4; ++k) { *(LAS f32x4*)(ro + k * 1024) = acc[4 + k]; keep[k] = acc[k]; }
    }
    bar_lgkm();
    const int c0 = 4 * (w & 1);
    unsigned o2[4][2];
#pragma unroll
    for (int k = 0; k < 4; ++k) {
      const f32x4 t = *(LAS f32x4*)(ri + k * 1024);
      const u32x2 uu = *(LAS u32x2*)(chl * USZ + (256 * (c0 + k) + 16 * i16 + 4 * g + 16) * 2);
      const unsigned u0 = uu[0], u1 = uu[1];
      o2[k][0] = pk2(keep[k][0] + t[0] + dsk * u2f(u0 << 16), keep[k][1] + t[1] + dsk * u2f(u0 & 0xFFFF0000u));
      o2[k][1] = pk2(keep[k][2] + t[2] + dsk * u2f(u1 << 16), keep[k][3] + t[3] + dsk * u2f(u1 & 0xFFFF0000u));
    }
#pragma unroll
    for (int k = 0; k < 2; ++k) {
      auto s0 = __builtin_amdgcn_permlane16_swap(o2[2 * k][0], o2[2 * k + 1][0], false, false);
      auto s1 = __builtin_amdgcn_permlane16_swap(o2[2 * k][1], o2[2 * k + 1][1], false, false);
      u32x4 v; v[0] = s0[0]; v[1] = s1[0]; v[2] = s0[1]; v[3] = s1[1];
      const int t = 256 * (c0 + 2 * k + (g & 1)) + 16 * i16 + 8 * (g >> 1);
      st16_sc1(ws, (t >> 6) * 65536 + (t & 63) * 2, WS_Y + ch * 128, v);
    }
  }
  wait_vm_bar<0>();
  if (tid == 0) st4_sc1(ws, 0, WS_FY + (xcd * 32 + bi) * FS, tag);
  STAMP(6);
  if (sidx == 0 && w == 0) {
#pragma unroll 1
    for (int k = 0; k < 4; ++k) wait_words(ws, WS_FX + (k * 64 + lane) * FS, tag);
    wait_words(ws, WS_L2 + (lane & 7) * 32 * 272 + 4, tag);
    if (lane == 0) st4_sc1(ws, 0, WS_E, epoch + 1u);
  }
  STAMP(7);
  STAMP_FLUSH();
}

NOPK __global__ void __launch_bounds__(256, 2) hh_s4_mix_kernel(Params p) {
  asm volatile("" : "+s"(p.x), "+s"(p.wqkv), "+s"(p.wphi), "+s"(p.wo), "+s"(p.gw), "+s"(p.out), "+s"(p.ws));
  asm volatile("" : "+s"(p.are), "+s"(p.aim), "+s"(p.bre), "+s"(p.bim), "+s"(p.cre), "+s"(p.cim), "+s"(p.logdt), "+s"(p.dskip));
  if (blockIdx.x < 256) att_block(p, blockIdx.x);
  else ssm_block(p, blockIdx.x - 256);
}

extern "C" void kernel_launch(void* const* d_in, const int* in_sizes, int n_in, void* d_out, int out_size, void* d_ws,
                              size_t ws_size, hipStream_t stream) {
  Params p;
  p.x = (const u16*)d_in[0]; p.wqkv = (const u16*)d_in[1]; p.wphi = (const u16*)d_in[2]; p.wo = (const u16*)d_in[3];
  p.are = (const u16*)d_in[5]; p.aim = (const u16*)d_in[6]; p.bre = (const u16*)d_in[7]; p.bim = (const u16*)d_in[8];
  p.cre = (const u16*)d_in[9]; p.cim = (const u16*)d_in[10]; p.logdt = (const u16*)d_in[11]; p.dskip = (const u16*)d_in[12];
  p.gw = (const u16*)d_in[13];
  p.out = (u16*)d_out;
  p.ws = (char*)d_ws;
  (void)hipFuncSetAttribute((const void*)hh_s4_mix_kernel, hipFuncAttributeMaxDynamicSharedMemorySize, LDS_BYTES);
  hh_s4_mix_kernel<<<512, 256, LDS_BYTES, stream>>>(p);
}
```

```cpp
#define TP 0
#include <hip/hip_runtime.h>
#include <hip/hip_bf16.h>
#include <stdint.h>
#pragma clang diagnostic ignored "-Wint-to-pointer-cast"

#define LAS __attribute__((address_space(3)))
#define GAS __attribute__((address_space(1)))
#if defined(__HIP_DEVICE_COMPILE__)
#define NOPK __attribute__((target("no-packed-fp32-ops")))
#else
#define NOPK
#endif
#define DI __device__ __forceinline__ NOPK
typedef __bf16 bf16x8 __attribute__((ext_vector_type(8)));
typedef __bf16 bf16x4 __attribute__((ext_vector_type(4)));
typedef __bf16 bf16x2 __attribute__((ext_vector_type(2)));
typedef float f32x16 __attribute__((ext_vector_type(16)));
typedef float f32x4 __attribute__((ext_vector_type(4)));
typedef float f32x2 __attribute__((ext_vector_type(2)));
typedef unsigned u32x4 __attribute__((ext_vector_type(4)));
typedef unsigned u32x2 __attribute__((ext_vector_type(2)));
typedef unsigned short u16;

#define FUSED 1
#define WARM 0

constexpr unsigned WS_E = 0;
constexpr unsigned FS = 128;
constexpr unsigned WS_FX = 4096;
constexpr unsigned WS_FY = WS_FX + 256 * FS;
constexpr unsigned WS_FO = WS_FY + 256 * FS;
constexpr unsigned WS_L2 = WS_FO + 256 * FS;
constexpr unsigned WS_L1Z = WS_L2 + 8 * 32 * 272;
constexpr unsigned WS_L1K = WS_L1Z + 8 * 32 * 512;
constexpr unsigned WS_XT = WS_L1K + 8 * 32 * 32 * 256;
constexpr unsigned WS_Y = WS_XT + 512 * 4096 + 4096;
constexpr unsigned WS_O = WS_Y + 32 * 512 * 128;
constexpr unsigned WS_ST = WS_O + 128 * 16 * 1024;
constexpr unsigned WS_END = WS_ST + 512 * 4 * 16 * 8;

constexpr unsigned SLOT = 16384;
constexpr unsigned A_XCH = 4 * SLOT;
constexpr unsigned A_WPHI = A_XCH + 8192;
constexpr unsigned A_GW = A_WPHI + 2048;
constexpr unsigned A_XZ = A_GW + 1024;
constexpr unsigned A_GP = A_XZ + 1024;
constexpr unsigned A_DUMMY = A_GP + 1024;
constexpr unsigned A_WO = 0, A_Y = A_XCH;
constexpr unsigned UCP = 4160, USZ = 4 * UCP;
constexpr unsigned S_KR = 2 * USZ, KSZ = 4608;
constexpr unsigned S_XT = S_KR + 2 * KSZ;
constexpr unsigned S_XCH = S_XT + 8192;
constexpr unsigned S_RED = S_XT;
constexpr unsigned LDS_BYTES = 79872;
constexpr int SPIN_CAP = 1 << 17;

struct Params {
  const u16 *x, *wqkv, *wphi, *wo, *are, *aim, *bre, *bim, *cre, *cim, *logdt, *dskip, *gw;
  u16* out;
  char* ws;
};

DI float u2f(unsigned u) { return __builtin_bit_cast(float, u); }
DI unsigned f2u(float f) { return __builtin_bit_cast(unsigned, f); }
DI bool wave_all(bool c) { return __builtin_amdgcn_ballot_w64(!c) == 0ull; }
DI unsigned pk2(float a, float b) { bf16x2 v; v[0] = (__bf16)a; v[1] = (__bf16)b; return __builtin_bit_cast(unsigned, v); }
DI float bf2f(u16 h) { return u2f((unsigned)h << 16); }
DI bf16x8 as_frag(u32x4 v) { return __builtin_bit_cast(bf16x8, v); }
DI bf16x8 pack8(const f32x16& a, int s) {
  u32x4 p;
  p[0] = pk2(a[8 * s + 0], a[8 * s + 1]); p[1] = pk2(a[8 * s + 2], a[8 * s + 3]);
  p[2] = pk2(a[8 * s + 4], a[8 * s + 5]); p[3] = pk2(a[8 * s + 6], a[8 * s + 7]);
  return as_frag(p);
}
DI void dma16(const void* sbase, unsigned voff, unsigned lds) {
  asm volatile("s_mov_b32 m0, %2\n\ts_nop 0\n\tglobal_load_lds_dwordx4 %0, %1" ::"v"(voff), "s"(sbase), "s"(lds) : "memory");
}
DI void dma16_sc1(const void* sbase, unsigned voff, unsigned lds) {
  asm volatile("s_mov_b32 m0, %2\n\ts_nop 0\n\tglobal_load_lds_dwordx4 %0, %1 sc1" ::"v"(voff), "s"(sbase), "s"(lds) : "memory");
}
DI void dma4(const void* sbase, unsigned voff, unsigned lds) {
  asm volatile("s_mov_b32 m0, %2\n\ts_nop 0\n\tglobal_load_lds_dword %0, %1" ::"v"(voff), "s"(sbase), "s"(lds) : "memory");
}
template <int LOFF> DI void dma16i(const void* sbase, unsigned voff, unsigned lbase) {
  asm volatile("s_add_u32 m0, %2, %3\n\ts_nop 0\n\tglobal_load_lds_dwordx4 %0, %1" ::"v"(voff), "s"(sbase), "s"(lbase), "n"(LOFF) : "memory", "scc");
}
#define SB() __builtin_amdgcn_sched_barrier(0)
#define PIN(x) asm volatile("" : "+v"(x))
DI float mul1(float a, float b) { return a * b; }
DI f32x2 pair(const f32x16& a, int i) { return f32x2{a[i], a[i + 1]}; }
DI void swap32(float& a, float& b) {
  auto r = __builtin_amdgcn_permlane32_swap(f2u(a), f2u(b), false, false);
  const unsigned r0 = r[0], r1 = r[1];
  a = u2f(r0); b = u2f(r1);
}
DI void swap16x4(u32x4& a, u32x4& b) {
#pragma unroll
  for (int d = 0; d < 4; ++d) {
    const unsigned x = a[d], y = b[d];
    auto r = __builtin_amdgcn_permlane16_swap(x, y, false, false);
    const unsigned r0 = r[0], r1 = r[1];
    a[d] = r0; b[d] = r1;
  }
}
template <int N> DI void wait_vm_bar() { asm volatile("s_waitcnt vmcnt(%0) lgkmcnt(0)\n\ts_barrier" ::"n"(N) : "memory"); }
DI void bar_lgkm() { asm volatile("s_waitcnt lgkmcnt(0)\n\ts_barrier" ::: "memory"); }
DI bf16x8 lds_frag(unsigned a) { return *(LAS bf16x8*)a; }
DI bf16x4 lds_tr(unsigned a) { return __builtin_amdgcn_ds_read_tr16_b64_v4bf16((LAS bf16x4*)a); }
DI bf16x8 lds_tr2(unsigned a0, unsigned a1) { return __builtin_shufflevector(lds_tr(a0), lds_tr(a1), 0, 1, 2, 3, 4, 5, 6, 7); }
#define MFMA32(a, b, c) __builtin_amdgcn_mfma_f32_32x32x16_bf16((a), (b), (c), 0, 0, 0)
#define MFMA16(a, b, c) __builtin_amdgcn_mfma_f32_16x16x32_bf16((a), (b), (c), 0, 0, 0)
DI float fsum32(float a) {
  auto r = __builtin_amdgcn_permlane32_swap(f2u(a), f2u(a), false, false);
  unsigned r0 = r[0], r1 = r[1];
  return u2f(r0) + u2f(r1);
}
DI float fmax32(float a) {
  auto r = __builtin_amdgcn_permlane32_swap(f2u(a), f2u(a), false, false);
  unsigned r0 = r[0], r1 = r[1];
  return __builtin_fmaxf(u2f(r0), u2f(r1));
}
DI float fmin32(float a) {
  auto r = __builtin_amdgcn_permlane32_swap(f2u(a), f2u(a), false, false);
  unsigned r0 = r[0], r1 = r[1];
  return __builtin_fminf(u2f(r0), u2f(r1));
}
DI float fsum16(float a) {
  auto r = __builtin_amdgcn_permlane16_swap(f2u(a), f2u(a), false, false);
  unsigned r0 = r[0], r1 = r[1];
  return u2f(r0) + u2f(r1);
}
DI float row_sum(float a) {
  a += u2f(__builtin_amdgcn_update_dpp(0u, f2u(a), 0x128, 0xF, 0xF, false));
  a += u2f(__builtin_amdgcn_update_dpp(0u, f2u(a), 0x124, 0xF, 0xF, false));
  a += u2f(__builtin_amdgcn_update_dpp(0u, f2u(a), 0x122, 0xF, 0xF, false));
  a += u2f(__builtin_amdgcn_update_dpp(0u, f2u(a), 0x121, 0xF, 0xF, false));
  return a;
}
DI unsigned load_tag(const char* ws) {
  unsigned e;
  asm volatile("s_load_dword %0, %1, 0x0\n\ts_waitcnt lgkmcnt(0)" : "=s"(e) : "s"(ws) : "memory");
  return e;
}
DI unsigned mk_tag(unsigned e) {
  const unsigned t = ((e + 1u) * 0x1E3779B1u + 0x2FEB352Du) & 0x3FFFFFFFu;
  return (t & 0x3FFFu) | ((t << 1) & 0x3FFF8000u) | ((t << 2) & 0x80000000u);
}
typedef __amdgpu_buffer_rsrc_t rsrc_t;
DI u32x4 ld16_sc1(rsrc_t r, unsigned vo, unsigned so = 0) { return __builtin_amdgcn_raw_buffer_load_b128(r, vo, so, 16); }
DI u32x2 ld8_sc1(rsrc_t r, unsigned vo, unsigned so = 0) { return __builtin_amdgcn_raw_buffer_load_b64(r, vo, so, 16); }
DI unsigned ld4_sc1(rsrc_t r, unsigned vo, unsigned so = 0) { return __builtin_amdgcn_raw_buffer_load_b32(r, vo, so, 16); }
DI void st16_sc1(rsrc_t r, unsigned vo, unsigned so, u32x4 v) { __builtin_amdgcn_raw_buffer_store_b128(v, r, vo + so, 0, 16); }
DI void st8_sc1(rsrc_t r, unsigned vo, unsigned so, u32x2 v) { __builtin_amdgcn_raw_buffer_store_b64(v, r, vo, so, 16); }
DI void st4_sc1(rsrc_t r, unsigned vo, unsigned so, unsigned v) { __builtin_amdgcn_raw_buffer_store_b32(v, r, vo, so, 16); }
DI void wait_words(rsrc_t r, unsigned off, unsigned tag) {
#pragma unroll 1
  for (int sp = 0; sp < SPIN_CAP; ++sp) {
    asm volatile("" ::: "memory");
    unsigned f = ld4_sc1(r, off);
    if (wave_all(f == tag)) break;
    __builtin_amdgcn_s_sleep(2);
  }
  asm volatile("" ::: "memory");
}

#define STAMP(k) do { if ((k) + role_ == TP) { __builtin_amdgcn_sched_barrier(0); asm volatile("s_memrealtime %0\n\ts_waitcnt lgkmcnt(0)" : "=s"(tp_)::"memory"); __builtin_amdgcn_sched_barrier(0); } } while (0)
#define STAMP_DECL unsigned long long tp_ = 0
#define STAMP_FLUSH() do { if (tp_) while (__builtin_amdgcn_s_memrealtime() - tp_ < 3000ull) __builtin_amdgcn_s_sleep(1); } while (0)

DI void softmax_exp(const f32x16& P, float (&a)[16], float (&b)[16]) {
  const float L = 1.44269504088896341f;
#define MX3(a, b, c) __builtin_fmaxf(__builtin_fmaxf(a, b), c)
#define MN3(a, b, c) __builtin_fminf(__builtin_fminf(a, b), c)
  float mx0 = MX3(MX3(MX3(MX3(P[0], P[1], P[2]), P[3], P[4]), P[5], P[6]), P[7], P[0]);
  float mx1 = MX3(MX3(MX3(MX3(P[8], P[9], P[10]), P[11], P[12]), P[13], P[14]), P[15], P[8]);
  float mn0 = MN3(MN3(MN3(MN3(P[0], P[1], P[2]), P[3], P[4]), P[5], P[6]), P[7], P[0]);
  float mn1 = MN3(MN3(MN3(MN3(P[8], P[9], P[10]), P[11], P[12]), P[13], P[14]), P[15], P[8]);
  swap32(mx0, mx1); swap32(mn0, mn1);
  mx0 = __builtin_fmaxf(mx0 * L, mx1 * L); mn0 = __builtin_fminf(mn0 * L, mn1 * L);
  mx1 = mx0; mn1 = mn0;
  swap32(mx0, mx1); swap32(mn0, mn1);
#pragma unroll
  for (int j = 0; j < 16; ++j) {
    a[j] = __builtin_amdgcn_exp2f(__builtin_fmaf(P[j], L, j < 8 ? -mx0 : -mx1));
    b[j] = __builtin_amdgcn_exp2f(__builtin_fmaf(P[j], -L, j < 8 ? mn0 : mn1));
  }
}
DI void softmax_rcp(const float (&a)[16], const float (&b)[16], float (&r)[4]) {
  float sa0 = a[0], sa1 = a[8], sb0 = b[0], sb1 = b[8];
#pragma unroll
  for (int j = 1; j < 8; ++j) { sa0 += a[j]; sa1 += a[8 + j]; sb0 += b[j]; sb1 += b[8 + j]; }
  swap32(sa0, sa1); swap32(sb0, sb1);
  sa0 = __builtin_amdgcn_rcpf(sa0 + sa1); sb0 = __builtin_amdgcn_rcpf(sb0 + sb1);
  sa1 = sa0; sb1 = sb0;
  swap32(sa0, sa1); swap32(sb0, sb1);
  r[0] = sa0; r[1] = sb0; r[2] = sa1; r[3] = sb1;
}
DI bf16x8 scale_pack(const float* v, float r) {
  return as_frag(u32x4{pk2(v[0] * r, v[1] * r), pk2(v[2] * r, v[3] * r), pk2(v[4] * r, v[5] * r), pk2(v[6] * r, v[7] * r)});
}

DI void att_block(const Params& p, int b) {
  constexpr int role_ = 0; (void)role_;
  STAMP_DECL;
  STAMP(0);
  const int tid = threadIdx.x, lane = tid & 63, w = __builtin_amdgcn_readfirstlane(tid >> 6);
  const int xcd = b & 7, bi = b >> 3, ti = 4 * xcd + (bi & 3), q = bi >> 2;
  const int th = w & 1, hp = w >> 1;
  const int rr = lane >> 3, sl = lane & 7;
  const unsigned vx0 = rr * 1024 + ((sl ^ (rr >> 1)) << 4), vx1 = (vx0 ^ 64u) + 8192;
  const unsigned vw0 = rr * 3072 + ((sl ^ (((rr >> 1) & 1) << 2)) << 4), vw1 = vw0 + 24576;
  const char* xs = (const char*)p.x + (unsigned)(64 * ti + 16 * w) * 1024u;
  const char* wsrc = (const char*)p.wqkv + (unsigned)(16 * w) * 3072u + 128 * q;
  const unsigned ldw = w * 2048;
#define XSRC(c) (xs + (((c) + q) & 7) * 128)
#define WSRC(c) (wsrc + (unsigned)(((c) + q) & 7) * 196608u)
#define DX(c, u) dma16i<((c) & 3) * SLOT + (u) * 1024>(XSRC(c), (u) ? vx1 : vx0, ldw)
#define DK(c, u) dma16i<((c) & 3) * SLOT + 8192 + (u) * 1024>(WSRC(c) + 1024, (u) ? vw1 : vw0, ldw)
#define DW(o, c, u) dma16i<(c) * 8192 + (u) * 1024>(WSRC(c) + (o), (u) ? vw1 : vw0, ldw)
  DX(0, 0); DX(0, 1); DK(0, 0); DK(0, 1);
  if (w < 2) dma16((const char*)p.wphi + q * 2048 + w * 1024, lane * 16, A_WPHI + w * 1024);
  else dma16((const char*)p.gw, lane * 16, A_GW);
  DX(1, 0); DX(1, 1); DK(1, 0); DK(1, 1);
  DX(2, 0); DX(2, 1); DK(2, 0); DK(2, 1);
  DX(3, 0); DX(3, 1); DK(3, 0); DK(3, 1);
  SB();
  __builtin_amdgcn_s_setprio(2);
  const int r = lane & 31, h = lane >> 5, blk = (lane >> 4) & 1, i16 = lane & 15, g4 = lane >> 4, qq = i16 >> 2, pp = lane & 3;
  unsigned tag = mk_tag(load_tag(p.ws + WS_E));
  asm volatile("" : "+s"(tag));
  const rsrc_t ws = __builtin_amdgcn_make_buffer_rsrc(p.ws, 0, WS_END, 0x00020000);
  unsigned xch = A_XCH + hp * 4096 + lane * 16, xz = A_XZ + hp * 256 + g4 * 64 + (i16 & 3) * 16;
  unsigned l1k = WS_L1K + ((q * 32 + 16 * hp + g4) * 32 + ti) * 256 + i16 * 16;
  unsigned l1z = WS_L1Z + ((q * 32 + 16 * hp + 4 * i16 + g4) * 32 + ti) * 16;
  unsigned gpa = A_GP + (32 * th + r) * 16 + (2 * hp + h) * 4;
  PIN(xch); PIN(xz); PIN(l1k); PIN(l1z); PIN(gpa);
  u32x4 SEL0, SEL1;
#pragma unroll
  for (int d = 0; d < 4; ++d) {
    const int f0 = ((2 * d) & 3) + 8 * (d >> 1) + 4 * h;
    SEL0[d] = (r == f0 ? 0x3F80u : 0u) | (r == f0 + 1 ? 0x3F800000u : 0u);
    SEL1[d] = (r == f0 + 16 ? 0x3F80u : 0u) | (r == f0 + 17 ? 0x3F800000u : 0u);
  }
  PIN(SEL0); PIN(SEL1);
  unsigned xa[4];
#pragma unroll
  for (int s = 0; s < 4; ++s) xa[s] = (32 * th + r) * 128 + (((2 * (s ^ (2 * hp)) + h) ^ ((r >> 1) & 7)) << 4);
  const unsigned wa_ = (8 * h + qq) * 128 + (((4 * hp + 2 * blk + (pp >> 1)) ^ ((qq >> 1) << 2)) << 4) + 8 * (pp & 1);
  unsigned waA = wa_ + 4096 * hp, waB = wa_ + 4096 - 4096 * hp;
  unsigned gaddr = A_GW + 64 * hp + 16 * h;
  PIN(waA); PIN(waB); PIN(gaddr);
#define WFR(base, s) lds_tr2((base) + ((s) < 2 ? waA : waB) + ((s) & 1) * 2048, (base) + ((s) < 2 ? waA : waB) + ((s) & 1) * 2048 + 512)

  bf16x8 X[32], wk[4];
  u32x4 gv[2];
  f32x16 ak;
  float ga = 0.f, gb = 0.f;
#pragma unroll
  for (int i = 0; i < 16; ++i) ak[i] = 0.f;
#define RDK(c, s)                                                                                     \
  do {                                                                                                \
    X[(4 * (c) + (s)) & 31] = lds_frag(((c) & 3) * SLOT + xa[s]);                                     \
    wk[s] = WFR(((c) & 3) * SLOT + 8192, s);                                                          \
  } while (0)
#define RDG(c, s) gv[s] = *(LAS u32x4*)(gaddr + ((((c) + q) & 7) * 128 + (s) * 32))
#define GATE(c, s)                                                                                    \
  do {                                                                                                \
    const u32x4 xv_ = __builtin_bit_cast(u32x4, X[4 * (c) + (s)]);                                    \
    const unsigned x0 = xv_[0], x1 = xv_[1], x2 = xv_[2], x3 = xv_[3], g0 = gv[s][0], g1 = gv[s][1], g2 = gv[s][2], g3 = gv[s][3]; \
    ga = __builtin_amdgcn_fdot2_f32_bf16(__builtin_bit_cast(bf16x2, x0), __builtin_bit_cast(bf16x2, g0), ga, false); \
    gb = __builtin_amdgcn_fdot2_f32_bf16(__builtin_bit_cast(bf16x2, x1), __builtin_bit_cast(bf16x2, g1), gb, false); \
    ga = __builtin_amdgcn_fdot2_f32_bf16(__builtin_bit_cast(bf16x2, x2), __builtin_bit_cast(bf16x2, g2), ga, false); \
    gb = __builtin_amdgcn_fdot2_f32_bf16(__builtin_bit_cast(bf16x2, x3), __builtin_bit_cast(bf16x2, g3), gb, false); \
    PIN(ga); PIN(gb);                                                                                 \
  } while (0)
  SB();
  wait_vm_bar<13>();
  STAMP(1);
  RDK(0, 0); RDK(0, 1); RDK(0, 2); RDK(0, 3);
#define MK(c, s)                                                                                      \
  do {                                                                                                \
    SB(); ak = MFMA32(wk[s], X[4 * (c) + (s)], ak); PIN(ak); SB();                                    \
    if ((c) < 7) RDK((c) + 1, s);                                                                     \
    SB();                                                                                             \
  } while (0)
#define ITER(c, D0, D1, D2, D3)                                                                       \
  do {                                                                                                \
    wait_vm_bar<8>();                                                                                 \
    D0; RDG(c, 0); RDG(c, 1); MK(c, 0); D1; MK(c, 1); D2; MK(c, 2); D3; MK(c, 3);                     \
    GATE(c, 0); GATE(c, 1); SB();                                                                     \
  } while (0)
  ITER(0, DX(4, 0), DX(4, 1), DK(4, 0), DK(4, 1));
  ITER(1, DX(5, 0), DX(5, 1), DK(5, 0), DK(5, 1));
  ITER(2, DX(6, 0), DX(6, 1), DK(6, 0), DK(6, 1));
  ITER(3, DX(7, 0), DX(7, 1), DK(7, 0), DK(7, 1));
  ITER(4, DW(2048, 0, 0), DW(2048, 0, 1), DW(2048, 1, 0), DW(2048, 1, 1));
  ITER(5, DW(2048, 2, 0), DW(2048, 2, 1), DW(2048, 3, 0), DW(2048, 3, 1));
  ITER(6, DW(2048, 4, 0), DW(2048, 4, 1), DW(2048, 5, 0), DW(2048, 5, 1));
  bf16x8 A1, A2;
  {
    const unsigned a_ = A_WPHI + (2 * hp + blk) * 512 + (4 * h + qq) * 32 + 8 * pp;
    u32x4 wf_;
    const u32x4 z_ = {0u, 0u, 0u, 0u};
    ITER(7, (DW(2048, 6, 0), wf_ = __builtin_bit_cast(u32x4, lds_tr2(a_, a_ + 256))), DW(2048, 6, 1),
         (DW(2048, 7, 0), A1 = as_frag(blk ? z_ : wf_), A2 = as_frag(blk ? wf_ : z_)), DW(2048, 7, 1));
    PIN(A1); PIN(A2);
  }
  STAMP(2);
  f32x16 zero16;
#pragma unroll
  for (int i = 0; i < 16; ++i) zero16[i] = 0.f;
  const f32x4 zero4 = {0.f, 0.f, 0.f, 0.f};
  {
    f32x16 P = MFMA32(A1, pack8(ak, 0), zero16);
    P = MFMA32(A2, pack8(ak, 1), P);
    float ea[16], eb[16], rc[4];
    softmax_exp(P, ea, eb);
#pragma unroll
    for (int j = 0; j < 16; ++j) { PIN(ea[j]); PIN(eb[j]); }
    SB();
    wait_vm_bar<8>();
    STAMP(12);
    f32x16 av = zero16;
    bf16x8 wv[4];
#define VRD(k) wv[(k) & 3] = WFR(((k) >> 2) * 8192, (k) & 3)
#define VMM(k) av = MFMA32(X[k], wv[(k) & 3], av)
    VRD(0); VRD(1); VRD(2);
    SB();
    bf16x8 f[2][2];
    softmax_rcp(ea, eb, rc);
    f[0][0] = scale_pack(ea, rc[0]); f[0][1] = scale_pack(eb, rc[1]);
    f[1][0] = scale_pack(ea + 8, rc[2]); f[1][1] = scale_pack(eb + 8, rc[3]);
#pragma unroll
    for (int k = 0; k < 16; ++k) { if (k + 3 < 16) VRD(k + 3); VMM(k); }
#pragma unroll
    for (int k = 0; k < 16; ++k) {
      __builtin_amdgcn_sched_group_barrier(0x100, 2, 0);
      __builtin_amdgcn_sched_group_barrier(0x008, 1, 0);
      __builtin_amdgcn_sched_group_barrier(0x002, 5, 0);
    }
    PIN(av); PIN(f[0][0]); PIN(f[0][1]); PIN(f[1][0]); PIN(f[1][1]);
    SB();
    wait_vm_bar<0>();
    VRD(16); VRD(17); VRD(18);
    u32x4 t0[2], t1[2], EZ[4];
    f32x16 T;
#define VG(k) SB(); VMM(k); PIN(av); SB(); if ((k) + 3 < 32) VRD((k) + 3)
#define TG(x) SB(); T = x; PIN(T); SB()
#define SW2(e, d) { const unsigned x_ = t0[e][d], y_ = t1[e][d], z_ = t0[e][(d) + 1], u_ = t1[e][(d) + 1];                       \
      auto r_ = __builtin_amdgcn_permlane16_swap(x_, y_, false, false); auto q_ = __builtin_amdgcn_permlane16_swap(z_, u_, false, false); \
      t0[e][d] = r_[0]; t1[e][d] = r_[1]; t0[e][(d) + 1] = q_[0]; t1[e][(d) + 1] = q_[1]; PIN(t0[e]); PIN(t1[e]); }
#define EZB(d) { const unsigned e_ = (i16 == (d)) ? 0x3F803F80u : 0u; EZ[d] = u32x4{e_, e_, e_, e_}; PIN(EZ[d]); }
    TG(MFMA32(f[0][0], as_frag(SEL0), zero16)); DW(0, 0, 0);
    TG(MFMA32(f[0][1], as_frag(SEL1), T)); DW(0, 0, 1);
    VG(16); DW(0, 1, 0);
    VG(17); t0[0] = __builtin_bit_cast(u32x4, pack8(T, 0)); PIN(t0[0]);
    VG(18); t1[0] = __builtin_bit_cast(u32x4, pack8(T, 1)); PIN(t1[0]);
    TG(MFMA32(f[1][0], as_frag(SEL0), zero16)); SW2(0, 0);
    TG(MFMA32(f[1][1], as_frag(SEL1), T)); SW2(0, 2);
    VG(19); DW(0, 1, 1);
    VG(20); DW(0, 2, 0);
    VG(21); t0[1] = __builtin_bit_cast(u32x4, pack8(T, 0)); PIN(t0[1]);
    VG(22); t1[1] = __builtin_bit_cast(u32x4, pack8(T, 1)); PIN(t1[1]);
    VG(23); SW2(1, 0);
    VG(24); SW2(1, 2);
    VG(25); DW(0, 2, 1);
    VG(26); EZB(0);
    VG(27); EZB(1);
    VG(28); DW(0, 3, 0); EZB(2);
    VG(29); DW(0, 3, 1); EZB(3);
    VG(30);
    VG(31);
    SB();
    f32x4 Z = MFMA16(as_frag(t0[0]), as_frag(EZ[0]), zero4);
    Z = MFMA16(as_frag(t1[0]), as_frag(EZ[1]), Z);
    Z = MFMA16(as_frag(t0[1]), as_frag(EZ[2]), Z);
    Z = MFMA16(as_frag(t1[1]), as_frag(EZ[3]), Z);
    PIN(Z);
    u32x4 vv[2] = {__builtin_bit_cast(u32x4, pack8(av, 0)), __builtin_bit_cast(u32x4, pack8(av, 1))};
    swap16x4(vv[0], vv[1]);
    f32x4 R[4];
    R[0] = MFMA16(as_frag(t0[0]), as_frag(vv[0]), zero4); R[1] = MFMA16(as_frag(t1[0]), as_frag(vv[0]), zero4);
    R[2] = MFMA16(as_frag(t0[1]), as_frag(vv[1]), zero4); R[3] = MFMA16(as_frag(t1[1]), as_frag(vv[1]), zero4);
    *(LAS float*)gpa = ga + gb;
    if (th) {
      if (i16 < 4) *(LAS f32x4*)xz = Z;
#pragma unroll
      for (int k = 0; k < 4; ++k) *(LAS f32x4*)(xch + k * 1024) = R[k];
      bar_lgkm();
    } else {
      bar_lgkm();
      f32x4 o[4];
#pragma unroll
      for (int k = 0; k < 4; ++k) o[k] = *(LAS f32x4*)(xch + k * 1024);
      const f32x4 oz = *(LAS f32x4*)xz;
#pragma unroll
      for (int k = 0; k < 4; ++k) {
        const f32x4 t = R[k] + o[k];
        st16_sc1(ws, l1k, k * 4 * 32 * 256, u32x4{pk2(t[0], t[1]), tag, pk2(t[2], t[3]), tag});
      }
      Z += oz;
      if (i16 < 4) st16_sc1(ws, l1z, 0, u32x4{pk2(Z[0], Z[1]), tag, pk2(Z[2], Z[3]), tag});
    }
  }
  SB();
  STAMP(3);
  const unsigned vwo = rr * 1024 + ((sl ^ (((rr >> 1) & 3) << 1)) << 4);
  const char* wos = (const char*)p.wo + 128 * q + (unsigned)w * 8192u;
#define DO(i) dma16i<A_WO + (i) * 4096>(wos + (i) * 32768, vwo, (unsigned)w * 1024u)
  bf16x8 pq[2][2];
  f32x16 PQ;
  {
    f32x16 aq = zero16;
    bf16x8 wq[8];
#define QRD(k) wq[(k) & 7] = WFR(((k) >> 2) * 8192, (k) & 3)
#define QG(k, k1) SB(); aq = MFMA32(wq[(k) & 7], X[k], aq); PIN(aq); SB(); if ((k) + 4 < (k1)) QRD((k) + 4)
#define DQ(c, u, j) dma16i<(c) * 8192 + (u) * 1024 + (j) * 2048 - 2048>(WSRC(c) + (j) * 49152 - 49152, (u) ? vw1 : vw0, ldw)
    if (th) {
      wait_vm_bar<0>();
      QRD(0); QRD(1); QRD(2); QRD(3);
      DQ(4, 0, 0); QG(0, 16); DQ(4, 1, 0); QG(1, 16); DQ(4, 0, 1); QG(2, 16); DQ(4, 1, 1); QG(3, 16);
      DQ(5, 0, 0); QG(4, 16); DQ(5, 1, 0); QG(5, 16); DQ(5, 0, 1); QG(6, 16); DQ(5, 1, 1); QG(7, 16);
      DQ(6, 0, 0); QG(8, 16); DQ(6, 1, 0); QG(9, 16); DQ(6, 0, 1); QG(10, 16); DQ(6, 1, 1); QG(11, 16);
      DQ(7, 0, 0); QG(12, 16); DQ(7, 1, 0); QG(13, 16); DQ(7, 0, 1); QG(14, 16); DQ(7, 1, 1); QG(15, 16);
      SB();
      STAMP(4);
      {
      const unsigned s01 = i16 == 0 ? 0x00003F80u : i16 == 1 ? 0x3F800000u : 0u, s23 = i16 == 2 ? 0x00003F80u : i16 == 3 ? 0x3F800000u : 0u;
      const bf16x8 SUM = as_frag(u32x4{s01, 0u, s23, 0u});
      const unsigned st = hp ? 16u : 256u;
      const unsigned o = g4 * 8 * st + (hp ? 0 : i16 * 16), so = hp ? WS_L1Z + (q * 32 + ti) * 512 : WS_L1K + (q * 32 + ti) * 8192;
      u32x4 g[8];
#pragma unroll 1
      for (int sp = 0; sp < SPIN_CAP; ++sp) {
        asm volatile("" ::: "memory");
        unsigned bad = 0;
#pragma unroll
        for (int i = 0; i < 8; ++i) g[i] = ld16_sc1(ws, o, so + i * st);
#pragma unroll
        for (int i = 0; i < 8; ++i) bad |= (g[i][1] ^ tag) | (g[i][3] ^ tag);
        if (wave_all(bad == 0)) break;
        __builtin_amdgcn_s_sleep(1);
      }
      f32x4 t = zero4;
#pragma unroll
      for (int i = 0; i < 8; ++i) t = MFMA16(SUM, as_frag(g[i]), t);
      if (lane < (hp ? 1 : 16)) st16_sc1(ws, i16 * 16, WS_L2 + (q * 32 + ti) * 272 + hp * 256, u32x4{pk2(t[0], t[1]), tag, pk2(t[2], t[3]), tag});
      }
      SB();
      STAMP(5);
      wait_vm_bar<1>();
    } else {
      wait_vm_bar<5>();
      QRD(0); QRD(1); QRD(2); QRD(3);
#pragma unroll
      for (int k = 0; k < 16; ++k) { QG(k, 16); }
      SB();
      bar_lgkm();
    }
    QRD(16); QRD(17); QRD(18); QRD(19);
    DO(0); QG(16, 32); QG(17, 32); DO(1); QG(18, 32); QG(19, 32); DO(2); QG(20, 32); QG(21, 32); DO(3); QG(22, 32); QG(23, 32);
    DO(4); QG(24, 32); QG(25, 32); DO(5); QG(26, 32); QG(27, 32); DO(6); QG(28, 32); QG(29, 32); DO(7); QG(30, 32); QG(31, 32);
    SB();

    PQ = MFMA32(A1, pack8(aq, 0), zero16);
    PQ = MFMA32(A2, pack8(aq, 1), PQ);
    PIN(PQ);
  }
  SB();
  SB();
  {
    float ea[16], eb[16], rc[4];
    softmax_exp(PQ, ea, eb);
    softmax_rcp(ea, eb, rc);
    pq[0][0] = scale_pack(ea, rc[0]); pq[0][1] = scale_pack(eb, rc[1]);
    pq[1][0] = scale_pack(ea + 8, rc[2]); pq[1][1] = scale_pack(eb + 8, rc[3]);
    PIN(pq[0][0]); PIN(pq[0][1]); PIN(pq[1][0]); PIN(pq[1][1]);
  }
  SB();
  STAMP(6);
  {
    unsigned o = h * 272 + (r < 16 ? r : 16) * 16, oo = blk * 16384 + (i16 + 16 * h) * 16;
    PIN(o); PIN(oo);
    const unsigned so = WS_L2 + (q * 32 + hp * 16) * 272, soo = WS_O + ((4 * ti + 2 * th) * 16 + 2 * q + hp) * 1024;
    u32x4 g[8];
#pragma unroll 1
    for (int sp = 0; sp < SPIN_CAP; ++sp) {
      asm volatile("" ::: "memory");
      unsigned bad = 0;
#pragma unroll
      for (int i = 0; i < 8; ++i) g[i] = ld16_sc1(ws, o + i * 544, so);
#pragma unroll
      for (int i = 0; i < 8; ++i) bad |= (g[i][1] ^ tag) | (g[i][3] ^ tag);
      if (wave_all(bad == 0)) break;
      __builtin_amdgcn_s_sleep(1);
    }
    STAMP(8);
    f32x16 Dn[2];
#pragma unroll
    for (int e = 0; e < 2; ++e) {
      const u32x4 f0 = {g[4 * e][0], g[4 * e][2], g[4 * e + 1][0], g[4 * e + 1][2]};
      const u32x4 f1 = {g[4 * e + 2][0], g[4 * e + 2][2], g[4 * e + 3][0], g[4 * e + 3][2]};
      Dn[e] = MFMA32(as_frag(f0), pq[e][0], zero16);
      Dn[e] = MFMA32(as_frag(f1), pq[e][1], Dn[e]);
    }
#pragma unroll
    for (int e = 0; e < 2; ++e) {
      const float rd = __builtin_amdgcn_rcpf(Dn[e][8] + 1e-6f);
      u32x4 ov;
#pragma unroll
      for (int j = 0; j < 4; ++j) ov[j] = pk2(mul1(Dn[e][2 * j], rd), mul1(Dn[e][2 * j + 1], rd));
      st16_sc1(ws, oo + e * 512, soo, ov);
    }
  }
  wait_vm_bar<0>();
  if (tid == 0) st4_sc1(ws, 0, WS_FO + (ti * 8 + q) * FS, tag);
  DO(8); DO(9); DO(10); DO(11); DO(12); DO(13); DO(14); DO(15);
  STAMP(9);
  {
    const int key = (2 * (g4 & 1) + (qq >> 1)) & 3;
    unsigned abase[4];
#pragma unroll
    for (int ct = 0; ct < 4; ++ct)
      abase[ct] = A_WO + (16 * (g4 >> 1) + 4 * (g4 & 1) + qq) * 128 + (((2 * ct + (pp >> 1)) ^ (key << 1)) << 4) + 8 * (pp & 1);
    bf16x8 A[8][4];
#pragma unroll
    for (int k = 0; k < 8; ++k)
#pragma unroll
      for (int ct = 0; ct < 4; ++ct) { A[k][ct] = lds_tr2(abase[ct] + k * 4096, abase[ct] + k * 4096 + 1024); }
    const f32x4 gp = *(LAS f32x4*)(A_GP + (16 * w + i16) * 16);
    float gate = __builtin_amdgcn_rcpf(1.f + __builtin_amdgcn_exp2f(-1.44269504088896341f * ((gp[0] + gp[1]) + (gp[2] + gp[3]))));
    PIN(gate);
#pragma unroll
    for (int k = 0; k < 8; ++k)
#pragma unroll
      for (int ct = 0; ct < 4; ++ct) PIN(A[k][ct]);
    const char* ysrc = p.ws + WS_Y + (unsigned)(ti * 512 + 64 * q + 16 * w) * 128u;
    const unsigned yvo = rr * 128 + ((sl ^ (((rr >> 1) & 3) << 1)) << 4);
    const unsigned obase = WS_O + (unsigned)(4 * ti + w) * 16384u;
    SB();
    const unsigned fo = lane < 32 ? WS_FY + (q * 32 + lane) * FS : WS_FO + (ti * 8 + (lane & 7)) * FS;
    unsigned long long miss;
#pragma unroll 1
    for (int sp = 0; sp < SPIN_CAP; ++sp) {
      asm volatile("" ::: "memory");
      miss = __builtin_amdgcn_ballot_w64(ld4_sc1(ws, fo) != tag);
      if ((miss >> 32) == 0) break;
      __builtin_amdgcn_s_sleep(1);
    }
    asm volatile("" ::: "memory");
    STAMP(10);
    if (miss == 0) { dma16_sc1(ysrc, yvo, A_Y + w * 2048); dma16_sc1(ysrc + 1024, yvo, A_Y + w * 2048 + 1024); }
    u32x4 of[16];
#pragma unroll
    for (int k = 0; k < 16; ++k) of[k] = ld16_sc1(ws, lane * 16 + (k & 3) * 1024, obase + (k >> 2) * 4096);
    SB();
    asm volatile("s_barrier" ::: "memory");
    f32x4 acc[4] = {zero4, zero4, zero4, zero4};
#pragma unroll
    for (int k = 0; k < 16; ++k) {
#pragma unroll
      for (int ct = 0; ct < 4; ++ct) {
        acc[ct] = MFMA16(A[k & 7][ct], as_frag(of[k]), acc[ct]);
        SB();
        if (k < 8) A[k][ct] = lds_tr2(abase[ct] + (k + 8) * 4096, abase[ct] + (k + 8) * 4096 + 1024);
        SB();
      }
    }
    if (miss != 0) {
      wait_words(ws, fo, tag);
      dma16_sc1(ysrc, yvo, A_Y + w * 2048); dma16_sc1(ysrc + 1024, yvo, A_Y + w * 2048 + 1024);
    }
    STAMP(7);
    wait_vm_bar<0>();
    const int ykey = (2 * g4 + (qq >> 1)) & 3;
    const unsigned ybase = A_Y + (4 * g4 + qq) * 128 + (((2 * w + (pp >> 1)) ^ (ykey << 1)) << 4) + 8 * (pp & 1);
    unsigned o2[4][2];
#pragma unroll
    for (int ct = 0; ct < 4; ++ct) {
      const bf16x4 yb = lds_tr(ybase + ct * 2048);
      float t[4];
#pragma unroll
      for (int j = 0; j < 4; ++j) { const float bv = (float)yb[j]; t[j] = bv + gate * (acc[ct][j] - bv); }
      o2[ct][0] = pk2(t[0], t[1]); o2[ct][1] = pk2(t[2], t[3]);
    }
    char* orow = (char*)p.out + (unsigned)(64 * ti + 16 * w + i16) * 1024u + 128 * q + 32 * (g4 & 1) + 16 * (g4 >> 1);
#pragma unroll
    for (int c2 = 0; c2 < 2; ++c2) {
      auto s0 = __builtin_amdgcn_permlane16_swap(o2[2 * c2][0], o2[2 * c2 + 1][0], false, false);
      auto s1 = __builtin_amdgcn_permlane16_swap(o2[2 * c2][1], o2[2 * c2 + 1][1], false, false);
      u32x4 v; v[0] = s0[0]; v[1] = s1[0]; v[2] = s0[1]; v[3] = s1[1];
      *(GAS u32x4*)(orow + 64 * c2) = v;
    }
  }
  STAMP(11);
  STAMP_FLUSH();
}

DI f32x2 cexp_l(float lr2, float li2, float l) {
  const float m = __builtin_amdgcn_exp2f(lr2 * l), ph = __builtin_amdgcn_fractf(li2 * l);
  return f32x2{m * __builtin_amdgcn_cosf(ph), m * __builtin_amdgcn_sinf(ph)};
}
#define BC "row_newbcast:%c5 row_mask:0xf bank_mask:0xf bound_ctrl:1"
template <int J> DI float bcmul(float x, float y) {
  float r;
  asm("v_mul_f32_dpp %0, %1, %2 row_newbcast:%3 row_mask:0xf bank_mask:0xf bound_ctrl:1" : "=v"(r) : "v"(x), "v"(y), "n"(J));
  return r;
}
template <int J> DI void bccmul(float ar, float ai, float br, float bi, float& re, float& im) {
  asm("v_mul_f32_dpp %0, %4, %2 row_newbcast:%6 row_mask:0xf bank_mask:0xf bound_ctrl:1\n\t"
      "v_mul_f32_dpp %1, %5, %2 row_newbcast:%6 row_mask:0xf bank_mask:0xf bound_ctrl:1\n\t"
      "v_fmac_f32_dpp %0, -%5, %3 row_newbcast:%6 row_mask:0xf bank_mask:0xf bound_ctrl:1\n\t"
      "v_fmac_f32_dpp %1, %4, %3 row_newbcast:%6 row_mask:0xf bank_mask:0xf bound_ctrl:1"
      : "=&v"(re), "=&v"(im) : "v"(ar), "v"(ai), "v"(br), "v"(bi), "n"(J));
}
struct Mode { float lr2, li2, wr, wi, gr, gi, fr, fi; };
template <int J> DI void kslot(const Mode& m, float rho, float r8, float r8n, unsigned& a0, unsigned& a1, unsigned& b0, unsigned& b1) {
  const float pa = __builtin_amdgcn_fractf(bcmul<J>(m.li2, rho)), pb = bcmul<J>(m.li2, r8n);
  const float ma = __builtin_amdgcn_exp2f(bcmul<J>(m.lr2, rho)), mb = __builtin_amdgcn_exp2f(bcmul<J>(m.lr2, r8));
  const float er = ma * __builtin_amdgcn_cosf(pa), ei = ma * __builtin_amdgcn_sinf(pa);
  const float yr = mb * __builtin_amdgcn_cosf(pb), yi = mb * __builtin_amdgcn_sinf(pb);
  float xr, xi, ur, ui, zr, zi;
  bccmul<J>(er, ei, m.wr, m.wi, xr, xi);
  bccmul<J>(yr, yi, m.fr, m.fi, zr, zi);
  bccmul<J>(xr, xi, m.gr, m.gi, ur, ui);
  a0 = pk2(xr, xi); a1 = pk2(ur, ui); b0 = pk2(yr, yi); b1 = pk2(zr, zi);
}
DI void split_pair(unsigned v, int h, float& lo_arr, float& hi_arr) {
  auto r = __builtin_amdgcn_permlane32_swap(v, v, false, false);
  const unsigned r0 = r[0], r1 = r[1], sh = h ? 0u : 16u;
  lo_arr = u2f((r0 << sh) & 0xFFFF0000u); hi_arr = u2f((r1 << sh) & 0xFFFF0000u);
}
DI float half_bf16(unsigned v, int idx) { return u2f((idx & 1) ? (v & 0xFFFF0000u) : (v << 16)); }

DI void ssm_block(const Params& p, int sidx) {
  constexpr int role_ = 100; (void)role_;
  STAMP_DECL;
  STAMP(0);
  const int tid = threadIdx.x, lane = tid & 63, w = __builtin_amdgcn_readfirstlane(tid >> 6);
  const int xcd = sidx & 7, bi = sidx >> 3;
  const int i16 = lane & 15, g = lane >> 4, qq = i16 >> 2, pp = lane & 3;
  const int chl = w >> 1, kh = w & 1, ch = 64 * xcd + 2 * bi + chl;
  __builtin_amdgcn_s_setprio(3);
  {
    const char* xs = (const char*)p.x + (unsigned)(64 * bi + 16 * w) * 1024u + 128 * xcd;
    const unsigned vo = (lane >> 3) * 1024 + (lane & 7) * 16;
    dma16(xs, vo, S_XT + w * 2048);
    dma16(xs + 8192, vo, S_XT + w * 2048 + 1024);
  }
  const int n = tid & 127, hh = lane >> 5;
  const unsigned po = (ch * 128 + 64 * kh) * 2 + (lane & 31) * 4;
  const unsigned pA = *(const GAS unsigned*)((const char*)(hh ? p.aim : p.are) + po);
  const unsigned pB = *(const GAS unsigned*)((const char*)(hh ? p.bim : p.bre) + po);
  const unsigned pC = *(const GAS unsigned*)((const char*)(hh ? p.cim : p.cre) + po);
  SB();
  __builtin_amdgcn_s_setprio(0);
  unsigned epoch, sdt, sds;
  asm volatile("s_load_dword %0, %3, 0x0\n\ts_load_dword %1, %4, 0x0\n\ts_load_dword %2, %5, 0x0\n\ts_waitcnt lgkmcnt(0)"
               : "=&s"(sdt), "=&s"(sds), "=&s"(epoch)
               : "s"((const char*)p.logdt + (ch >> 1) * 4), "s"((const char*)p.dskip + (ch >> 1) * 4), "s"(p.ws + WS_E) : "memory");
  const float ldt = half_bf16(sdt, ch), dsk = half_bf16(sds, ch);
  unsigned tag = mk_tag(epoch);
  asm volatile("" : "+s"(tag));
  const rsrc_t ws = __builtin_amdgcn_make_buffer_rsrc(p.ws, 0, WS_END, 0x00020000);
  const int r2_ = i16 == 11 ? 8 : 480 + 4 * i16, r3_ = 1024 + 8 * (i16 & 1) + 256 * (i16 & 2);
  float rho = (float)(i16 < 8 ? i16 : i16 < 12 ? r2_ : r3_);
  float r8 = 8.f * i16, r8n = -r8;
  PIN(rho); PIN(r8); PIN(r8n);
  const float dt = (float)(__bf16)__builtin_expf(ldt);
  {
    unsigned pa_ = pA, pb_ = pB, pc_ = pC;
    PIN(pa_); PIN(pb_); PIN(pc_);
  }
  STAMP(1);
  Mode md;
  {
    float are, aim, bre, bim, cre, cim;
    split_pair(pA, hh, are, aim); split_pair(pB, hh, bre, bim); split_pair(pC, hh, cre, cim);
    md.lr2 = dt * are * 1.44269504088896341f;
    md.li2 = dt * aim * 0.15915494309189535f;
    md.li2 -= __builtin_rintf(md.li2);
    const float dbr = dt * bre, dbi = dt * bim;
    md.wr = cre * dbr - cim * dbi; md.wi = cre * dbi + cim * dbr;
    const f32x2 G = cexp_l(md.lr2, md.li2, 256.f), F = cexp_l(md.lr2, -md.li2, 128.f);
    md.gr = G[0]; md.gi = G[1]; md.fr = F[0]; md.fi = F[1];
    asm volatile("s_nop 1" : "+v"(md.lr2), "+v"(md.li2), "+v"(md.wr), "+v"(md.wi), "+v"(md.gr), "+v"(md.gi), "+v"(md.fr), "+v"(md.fi));
  }
  const f32x4 zero4 = {0.f, 0.f, 0.f, 0.f};
  f32x4 D[2][2] = {{zero4, zero4}, {zero4, zero4}};
#define KSTEP(s)                                                                                      \
  do {                                                                                                \
    unsigned a0[4], a1[4], b0[4], b1[4];                                                              \
    kslot<4 * (s) + 0>(md, rho, r8, r8n, a0[0], a1[0], b0[0], b1[0]);                        \
    kslot<4 * (s) + 1>(md, rho, r8, r8n, a0[1], a1[1], b0[1], b1[1]);                        \
    kslot<4 * (s) + 2>(md, rho, r8, r8n, a0[2], a1[2], b0[2], b1[2]);                        \
    kslot<4 * (s) + 3>(md, rho, r8, r8n, a0[3], a1[3], b0[3], b1[3]);                        \
    const bf16x8 A0 = as_frag(u32x4{a0[0], a0[1], a0[2], a0[3]}), A1 = as_frag(u32x4{a1[0], a1[1], a1[2], a1[3]}); \
    const bf16x8 B0 = as_frag(u32x4{b0[0], b0[1], b0[2], b0[3]}), B1 = as_frag(u32x4{b1[0], b1[1], b1[2], b1[3]}); \
    D[0][0] = MFMA16(A0, B0, D[0][0]); D[0][1] = MFMA16(A0, B1, D[0][1]);                             \
    D[1][0] = MFMA16(A1, B0, D[1][0]); D[1][1] = MFMA16(A1, B1, D[1][1]);                             \
    PIN(D[0][0]); PIN(D[0][1]); PIN(D[1][0]); PIN(D[1][1]);                                           \
  } while (0)
  KSTEP(0);
  wait_vm_bar<WARM ? 2 : 0>();
  {
    const unsigned a = S_XT + (8 * g + qq) * 128 + (16 * w + 4 * pp) * 2;
    const unsigned vo = i16 * 4096 + g * 16, so = WS_XT + (64 * xcd + 16 * w) * 4096 + 128 * bi;
    st16_sc1(ws, vo, so, __builtin_bit_cast(u32x4, lds_tr2(a, a + 512)));
    st16_sc1(ws, vo + 64, so, __builtin_bit_cast(u32x4, lds_tr2(a + 4096, a + 4096 + 512)));
  }
  SB();
  KSTEP(1);
  KSTEP(2);
  wait_vm_bar<0>();
  if (tid == 0) st4_sc1(ws, 0, WS_FX + (xcd * 32 + bi) * FS, tag);
  STAMP(8);
  KSTEP(3);
  STAMP(2);
  *(LAS f32x4*)(S_XCH + w * 2048 + lane * 16) = kh ? D[0][0] : D[0][1];
  *(LAS f32x4*)(S_XCH + w * 2048 + 1024 + lane * 16) = kh ? D[1][0] : D[1][1];
  wait_words(ws, WS_FX + (xcd * 32 + (lane & 31)) * FS, tag);
  asm volatile("" ::: "memory");
  const u32x4 ua_ = ld16_sc1(ws, n * 32, WS_XT + ch * 4096), ub_ = ld16_sc1(ws, n * 32 + 16, WS_XT + ch * 4096);
  const u32x2 uc_ = ld8_sc1(ws, n * 32 + 32, WS_XT + ch * 4096);
  STAMP(9);
  bar_lgkm();
  {
    const f32x4 k0 = (kh ? D[0][1] : D[0][0]) + *(LAS f32x4*)(S_XCH + (w ^ 1) * 2048 + lane * 16);
    const f32x4 k1 = (kh ? D[1][1] : D[1][0]) + *(LAS f32x4*)(S_XCH + (w ^ 1) * 2048 + 1024 + lane * 16);
    float t0 = k1[0], t1 = t0, u0 = k1[3], u1 = u0;
    { auto r_ = __builtin_amdgcn_permlane16_swap(f2u(t0), f2u(t1), false, false); const unsigned x_ = r_[1]; t1 = u2f(x_); }
    swap32(u0, u1); u0 = u1;
    { auto r_ = __builtin_amdgcn_permlane16_swap(f2u(u0), f2u(u1), false, false); const unsigned x_ = r_[0]; u0 = u2f(x_); }
    const unsigned ka = S_KR + chl * KSZ + (248 - 128 * kh - 8 * i16) * 2;
    if (g < 2) {
      const float nx = g ? u0 : t1;
      *(LAS u32x2*)(ka + 2 * 1792 + 8 - 8 * g) = u32x2{pk2(k0[3], k0[2]), pk2(k0[1], k0[0])};
      *(LAS u32x2*)(ka + 2 * 1536 + 8 - 8 * g) = u32x2{pk2(nx, k1[2]), pk2(k1[0], k1[0])};
    } else if (g == 2) {
      *(LAS u32x4*)(ka + 2 * 1280) = u32x4{pk2(k0[2], k0[2]), pk2(k0[1], k0[1]), pk2(k0[1], k0[0]), pk2(k0[0], k0[0])};
      *(LAS u32x4*)(ka + 2 * 1024) = u32x4{pk2(k1[2], k1[2]), pk2(k1[1], k1[1]), pk2(k1[1], k1[0]), pk2(k1[0], k1[0])};
    } else {
      const bool odd = i16 & 1;
      const unsigned c0 = pk2(k0[0], k0[0]), c1 = pk2(k1[0], k1[0]), c2 = pk2(k0[2], k0[2]), c3 = pk2(k1[2], k1[2]);
      *(LAS u32x4*)(ka + 2 * 768) = u32x4{pk2(k0[1], k0[1]), pk2(k0[1], odd ? k0[1] : k0[0]), c0, c0};
      *(LAS u32x4*)(ka + 2 * 512) = u32x4{pk2(k1[1], k1[1]), pk2(k1[1], odd ? k1[1] : k1[0]), c1, c1};
      *(LAS u32x4*)(ka + 2 * 256) = u32x4{pk2(k0[3], k0[3]), pk2(k0[3], odd ? k0[3] : k0[2]), c2, c2};
      *(LAS u32x4*)(ka) = u32x4{pk2(k1[3], k1[3]), pk2(k1[3], odd ? k1[3] : k1[2]), c3, c3};
    }
    if (kh == 0 && lane < 30) *(LAS u32x4*)(S_KR + chl * KSZ + 4096 + lane * 16) = u32x4{0u, 0u, 0u, 0u};
  }
  STAMP(3);
  {
    const unsigned d[10] = {ua_[0], ua_[1], ua_[2], ua_[3], ub_[0], ub_[1], ub_[2], ub_[3], uc_[0], uc_[1]};
    unsigned e[9];
#pragma unroll
    for (int k = 0; k < 9; ++k) e[k] = __builtin_amdgcn_alignbit(d[k + 1], d[k], 16);
    const unsigned u = chl * USZ + 32 + n * 32;
    *(LAS u32x4*)(u) = ua_; *(LAS u32x4*)(u + 16) = ub_;
    *(LAS u32x4*)(u + UCP) = u32x4{e[0], e[1], e[2], e[3]}; *(LAS u32x4*)(u + UCP + 16) = u32x4{e[4], e[5], e[6], e[7]};
    *(LAS u32x4*)(u + 2 * UCP) = u32x4{d[1], d[2], d[3], d[4]}; *(LAS u32x4*)(u + 2 * UCP + 16) = u32x4{d[5], d[6], d[7], d[8]};
    *(LAS u32x4*)(u + 3 * UCP) = u32x4{e[1], e[2], e[3], e[4]}; *(LAS u32x4*)(u + 3 * UCP + 16) = u32x4{e[5], e[6], e[7], e[8]};
    if (n == 0) {
      const u32x4 z = {0u, 0u, 0u, 0u};
      const unsigned u0 = chl * USZ;
      *(LAS u32x4*)(u0) = z; *(LAS u32x4*)(u0 + 16) = z;
      *(LAS u32x4*)(u0 + UCP) = z; *(LAS u32x4*)(u0 + UCP + 16) = u32x4{0u, 0u, 0u, d[0] << 16};
      *(LAS u32x4*)(u0 + 2 * UCP) = z; *(LAS u32x4*)(u0 + 2 * UCP + 16) = u32x4{0u, 0u, 0u, d[0]};
      *(LAS u32x4*)(u0 + 3 * UCP) = z; *(LAS u32x4*)(u0 + 3 * UCP + 16) = u32x4{0u, 0u, d[0] << 16, e[0]};
    }
  }
  bar_lgkm();
  STAMP(4);
  f32x4 acc[8];
#pragma unroll
  for (int c = 0; c < 8; ++c) acc[c] = f32x4{0.f, 0.f, 0.f, 0.f};
  {
    const int sg = (i16 + 1) & 3;
    const unsigned ua = chl * USZ + sg * UCP + (i16 + 1 - sg + 8 * g) * 2 + (w & 1) * 256;
    const unsigned kb = S_KR + chl * KSZ + (240 - 16 * i16 + 8 * g) * 2 + (w & 1) * 256;
    unsigned ub = ua + 8;
    PIN(ub);
    u32x2 Al[2][8], Ah[2][8];
    bf16x8 G[2][8];
#define CRD(bf, rho, i)                                                                               \
  do {                                                                                                \
    if ((i) % 3 == 0) Al[bf][(i) / 3] = *(LAS u32x2*)(ua + 64 * (rho) + 512 * ((i) / 3));             \
    else if ((i) % 3 == 1) Ah[bf][(i) / 3] = *(LAS u32x2*)(ub + 64 * (rho) + 512 * ((i) / 3));        \
    else G[bf][(i) / 3] = lds_frag(kb + 64 * (rho) + 512 * (7 - (i) / 3));                            \
  } while (0)
#pragma unroll
    for (int i = 0; i < 24; ++i) { CRD(0, 0, i); SB(); }
#pragma unroll
    for (int rho = 0; rho < 4; ++rho) {
      int cnt = 0;
#pragma unroll
      for (int dsum = 0; dsum < 8; ++dsum) {
#pragma unroll
        for (int k = 0; k <= 2 * dsum; ++k) {
          const int m = k <= dsum ? k : dsum, nn = k <= dsum ? dsum : 2 * dsum - k;
          if (m + nn < 8) {
            SB();
            const u32x4 af = {Al[rho & 1][m][0], Al[rho & 1][m][1], Ah[rho & 1][m][0], Ah[rho & 1][m][1]};
            acc[m + nn] = MFMA16(as_frag(af), G[rho & 1][nn], acc[m + nn]);
            PIN(acc[m + nn]);
            SB();
            if (rho < 3 && cnt % 3 != 2 && cnt - cnt / 3 < 24) CRD((rho + 1) & 1, rho + 1, cnt - cnt / 3);
            ++cnt;
          }
        }
      }
    }
    SB();
  }
  STAMP(5);
  {
    f32x4 keep[4];
    const unsigned ro = S_RED + w * 4096 + lane * 16, ri = S_RED + (w ^ 1) * 4096 + lane * 16;
    if (w & 1) {
#pragma unroll
      for (int k = 0; k < 4; ++k) { *(LAS f32x4*)(ro + k * 1024) = acc[k]; keep[k] = acc[4 + k]; }
    } else {
#pragma unroll
      for (int k = 0; k < 4; ++k) { *(LAS f32x4*)(ro + k * 1024) = acc[4 + k]; keep[k] = acc[k]; }
    }
    bar_lgkm();
    const int c0 = 4 * (w & 1);
    unsigned o2[4][2];
#pragma unroll
    for (int k = 0; k < 4; ++k) {
      const f32x4 t = *(LAS f32x4*)(ri + k * 1024);
      const u32x2 uu = *(LAS u32x2*)(chl * USZ + (256 * (c0 + k) + 16 * i16 + 4 * g + 16) * 2);
      const unsigned u0 = uu[0], u1 = uu[1];
      o2[k][0] = pk2(keep[k][0] + t[0] + dsk * u2f(u0 << 16), keep[k][1] + t[1] + dsk * u2f(u0 & 0xFFFF0000u));
      o2[k][1] = pk2(keep[k][2] + t[2] + dsk * u2f(u1 << 16), keep[k][3] + t[3] + dsk * u2f(u1 & 0xFFFF0000u));
    }
#pragma unroll
    for (int k = 0; k < 2; ++k) {
      auto s0 = __builtin_amdgcn_permlane16_swap(o2[2 * k][0], o2[2 * k + 1][0], false, false);
      auto s1 = __builtin_amdgcn_permlane16_swap(o2[2 * k][1], o2[2 * k + 1][1], false, false);
      u32x4 v; v[0] = s0[0]; v[1] = s1[0]; v[2] = s0[1]; v[3] = s1[1];
      const int t = 256 * (c0 + 2 * k + (g & 1)) + 16 * i16 + 8 * (g >> 1);
      st16_sc1(ws, (t >> 6) * 65536 + (t & 63) * 2, WS_Y + ch * 128, v);
    }
  }
  wait_vm_bar<0>();
  if (tid == 0) st4_sc1(ws, 0, WS_FY + (xcd * 32 + bi) * FS, tag);
  STAMP(6);
  if (sidx == 0 && w == 0) {
#pragma unroll 1
    for (int k = 0; k < 4; ++k) wait_words(ws, WS_FX + (k * 64 + lane) * FS, tag);
    wait_words(ws, WS_L2 + (lane & 7) * 32 * 272 + 4, tag);
    if (lane == 0) st4_sc1(ws, 0, WS_E, epoch + 1u);
  }
  STAMP(7);
  STAMP_FLUSH();
}

NOPK __global__ void __launch_bounds__(256, 2) hh_s4_mix_kernel(Params p) {
  asm volatile("" : "+s"(p.x), "+s"(p.wqkv), "+s"(p.wphi), "+s"(p.wo), "+s"(p.gw), "+s"(p.out), "+s"(p.ws));
  asm volatile("" : "+s"(p.are), "+s"(p.aim), "+s"(p.bre), "+s"(p.bim), "+s"(p.cre), "+s"(p.cim), "+s"(p.logdt), "+s"(p.dskip));
  if (blockIdx.x < 256) att_block(p, blockIdx.x);
  else ssm_block(p, blockIdx.x - 256);
}

extern "C" void kernel_launch(void* const* d_in, const int* in_sizes, int n_in, void* d_out, int out_size, void* d_ws,
                              size_t ws_size, hipStream_t stream) {
  Params p;
  p.x = (const u16*)d_in[0]; p.wqkv = (const u16*)d_in[1]; p.wphi = (const u16*)d_in[2]; p.wo = (const u16*)d_in[3];
  p.are = (const u16*)d_in[5]; p.aim = (const u16*)d_in[6]; p.bre = (const u16*)d_in[7]; p.bim = (const u16*)d_in[8];
  p.cre = (const u16*)d_in[9]; p.cim = (const u16*)d_in[10]; p.logdt = (const u16*)d_in[11]; p.dskip = (const u16*)d_in[12];
  p.gw = (const u16*)d_in[13];
  p.out = (u16*)d_out;
  p.ws = (char*)d_ws;
  (void)hipFuncSetAttribute((const void*)hh_s4_mix_kernel, hipFuncAttributeMaxDynamicSharedMemorySize, LDS_BYTES);
  hh_s4_mix_kernel<<<512, 256, LDS_BYTES, stream>>>(p);
}
```

```cpp
#include <hip/hip_runtime.h>
#include <hip/hip_bf16.h>
#include <stdint.h>
#pragma clang diagnostic ignored "-Wint-to-pointer-cast"

#define LAS __attribute__((address_space(3)))
#define GAS __attribute__((address_space(1)))
#if defined(__HIP_DEVICE_COMPILE__)
#define NOPK __attribute__((target("no-packed-fp32-ops")))
#else
#define NOPK
#endif
#define DI __device__ __forceinline__ NOPK
typedef __bf16 bf16x8 __attribute__((ext_vector_type(8)));
typedef __bf16 bf16x4 __attribute__((ext_vector_type(4)));
typedef __bf16 bf16x2 __attribute__((ext_vector_type(2)));
typedef float f32x16 __attribute__((ext_vector_type(16)));
typedef float f32x4 __attribute__((ext_vector_type(4)));
typedef float f32x2 __attribute__((ext_vector_type(2)));
typedef unsigned u32x4 __attribute__((ext_vector_type(4)));
typedef unsigned u32x2 __attribute__((ext_vector_type(2)));
typedef unsigned short u16;

#define FUSED 1
#define WARM 0

constexpr unsigned WS_E = 0;
constexpr unsigned FS = 128;
constexpr unsigned WS_FX = 4096;
constexpr unsigned WS_FY = WS_FX + 256 * FS;
constexpr unsigned WS_FO = WS_FY + 256 * FS;
constexpr unsigned WS_L2 = WS_FO + 256 * FS;
constexpr unsigned WS_L1Z = WS_L2 + 8 * 32 * 272;
constexpr unsigned WS_L1K = WS_L1Z + 8 * 32 * 512;
constexpr unsigned WS_XT = WS_L1K + 8 * 32 * 32 * 256;
constexpr unsigned WS_Y = WS_XT + 512 * 4096 + 4096;
constexpr unsigned WS_O = WS_Y + 32 * 512 * 128;
constexpr unsigned WS_ST = WS_O + 128 * 16 * 1024;
constexpr unsigned WS_END = WS_ST + 512 * 4 * 16 * 8;

constexpr unsigned SLOT = 16384;
constexpr unsigned A_XCH = 4 * SLOT;
constexpr unsigned A_WPHI = A_XCH + 8192;
constexpr unsigned A_GW = A_WPHI + 2048;
constexpr unsigned A_XZ = A_GW + 1024;
constexpr unsigned A_GP = A_XZ + 1024;
constexpr unsigned A_DUMMY = A_GP + 1024;
constexpr unsigned A_WO = 0, A_Y = A_XCH;
constexpr unsigned UCP = 4160, USZ = 4 * UCP;
constexpr unsigned S_KR = 2 * USZ, KSZ = 4608;
constexpr unsigned S_XT = S_KR + 2 * KSZ;
constexpr unsigned S_XCH = S_XT + 8192;
constexpr unsigned S_RED = S_XT;
constexpr unsigned LDS_BYTES = 79872;
constexpr int SPIN_CAP = 1 << 17;

struct Params {
  const u16 *x, *wqkv, *wphi, *wo, *are, *aim, *bre, *bim, *cre, *cim, *logdt, *dskip, *gw;
  u16* out;
  char* ws;
};

DI float u2f(unsigned u) { return __builtin_bit_cast(float, u); }
DI unsigned f2u(float f) { return __builtin_bit_cast(unsigned, f); }
DI bool wave_all(bool c) { return __builtin_amdgcn_ballot_w64(!c) == 0ull; }
DI unsigned pk2(float a, float b) { bf16x2 v; v[0] = (__bf16)a; v[1] = (__bf16)b; return __builtin_bit_cast(unsigned, v); }
DI float bf2f(u16 h) { return u2f((unsigned)h << 16); }
DI bf16x8 as_frag(u32x4 v) { return __builtin_bit_cast(bf16x8, v); }
DI bf16x8 pack8(const f32x16& a, int s) {
  u32x4 p;
  p[0] = pk2(a[8 * s + 0], a[8 * s + 1]); p[1] = pk2(a[8 * s + 2], a[8 * s + 3]);
  p[2] = pk2(a[8 * s + 4], a[8 * s + 5]); p[3] = pk2(a[8 * s + 6], a[8 * s + 7]);
  return as_frag(p);
}
DI void dma16(const void* sbase, unsigned voff, unsigned lds) {
  asm volatile("s_mov_b32 m0, %2\n\ts_nop 0\n\tglobal_load_lds_dwordx4 %0, %1" ::"v"(voff), "s"(sbase), "s"(lds) : "memory");
}
DI void dma16_sc1(const void* sbase, unsigned voff, unsigned lds) {
  asm volatile("s_mov_b32 m0, %2\n\ts_nop 0\n\tglobal_load_lds_dwordx4 %0, %1 sc1" ::"v"(voff), "s"(sbase), "s"(lds) : "memory");
}
DI void dma4(const void* sbase, unsigned voff, unsigned lds) {
  asm volatile("s_mov_b32 m0, %2\n\ts_nop 0\n\tglobal_load_lds_dword %0, %1" ::"v"(voff), "s"(sbase), "s"(lds) : "memory");
}
template <int LOFF> DI void dma16i(const void* sbase, unsigned voff, unsigned lbase) {
  asm volatile("s_add_u32 m0, %2, %3\n\ts_nop 0\n\tglobal_load_lds_dwordx4 %0, %1" ::"v"(voff), "s"(sbase), "s"(lbase), "n"(LOFF) : "memory", "scc");
}
#define SB() __builtin_amdgcn_sched_barrier(0)
#define PIN(x) asm volatile("" : "+v"(x))
DI float mul1(float a, float b) { return a * b; }
DI f32x2 pair(const f32x16& a, int i) { return f32x2{a[i], a[i + 1]}; }
DI void swap32(float& a, float& b) {
  auto r = __builtin_amdgcn_permlane32_swap(f2u(a), f2u(b), false, false);
  const unsigned r0 = r[0], r1 = r[1];
  a = u2f(r0); b = u2f(r1);
}
DI void swap16x4(u32x4& a, u32x4& b) {
#pragma unroll
  for (int d = 0; d < 4; ++d) {
    const unsigned x = a[d], y = b[d];
    auto r = __builtin_amdgcn_permlane16_swap(x, y, false, false);
    const unsigned r0 = r[0], r1 = r[1];
    a[d] = r0; b[d] = r1;
  }
}
template <int N> DI void wait_vm_bar() { asm volatile("s_waitcnt vmcnt(%0) lgkmcnt(0)\n\ts_barrier" ::"n"(N) : "memory"); }
DI void bar_lgkm() { asm volatile("s_waitcnt lgkmcnt(0)\n\ts_barrier" ::: "memory"); }
DI bf16x8 lds_frag(unsigned a) { return *(LAS bf16x8*)a; }
DI bf16x4 lds_tr(unsigned a) { return __builtin_amdgcn_ds_read_tr16_b64_v4bf16((LAS bf16x4*)a); }
DI bf16x8 lds_tr2(unsigned a0, unsigned a1) { return __builtin_shufflevector(lds_tr(a0), lds_tr(a1), 0, 1, 2, 3, 4, 5, 6, 7); }
#define MFMA32(a, b, c) __builtin_amdgcn_mfma_f32_32x32x16_bf16((a), (b), (c), 0, 0, 0)
#define MFMA16(a, b, c) __builtin_amdgcn_mfma_f32_16x16x32_bf16((a), (b), (c), 0, 0, 0)
DI float fsum32(float a) {
  auto r = __builtin_amdgcn_permlane32_swap(f2u(a), f2u(a), false, false);
  unsigned r0 = r[0], r1 = r[1];
  return u2f(r0) + u2f(r1);
}
DI float fmax32(float a) {
  auto r = __builtin_amdgcn_permlane32_swap(f2u(a), f2u(a), false, false);
  unsigned r0 = r[0], r1 = r[1];
  return __builtin_fmaxf(u2f(r0), u2f(r1));
}
DI float fmin32(float a) {
  auto r = __builtin_amdgcn_permlane32_swap(f2u(a), f2u(a), false, false);
  unsigned r0 = r[0], r1 = r[1];
  return __builtin_fminf(u2f(r0), u2f(r1));
}
DI float fsum16(float a) {
  auto r = __builtin_amdgcn_permlane16_swap(f2u(a), f2u(a), false, false);
  unsigned r0 = r[0], r1 = r[1];
  return u2f(r0) + u2f(r1);
}
DI float row_sum(float a) {
  a += u2f(__builtin_amdgcn_update_dpp(0u, f2u(a), 0x128, 0xF, 0xF, false));
  a += u2f(__builtin_amdgcn_update_dpp(0u, f2u(a), 0x124, 0xF, 0xF, false));
  a += u2f(__builtin_amdgcn_update_dpp(0u, f2u(a), 0x122, 0xF, 0xF, false));
  a += u2f(__builtin_amdgcn_update_dpp(0u, f2u(a), 0x121, 0xF, 0xF, false));
  return a;
}
DI unsigned load_tag(const char* ws) {
  unsigned e;
  asm volatile("s_load_dword %0, %1, 0x0\n\ts_waitcnt lgkmcnt(0)" : "=s"(e) : "s"(ws) : "memory");
  return e;
}
DI unsigned mk_tag(unsigned e) {
  const unsigned t = ((e + 1u) * 0x1E3779B1u + 0x2FEB352Du) & 0x3FFFFFFFu;
  return (t & 0x3FFFu) | ((t << 1) & 0x3FFF8000u) | ((t << 2) & 0x80000000u);
}
typedef __amdgpu_buffer_rsrc_t rsrc_t;
DI u32x4 ld16_sc1(rsrc_t r, unsigned vo, unsigned so = 0) { return __builtin_amdgcn_raw_buffer_load_b128(r, vo, so, 16); }
DI u32x2 ld8_sc1(rsrc_t r, unsigned vo, unsigned so = 0) { return __builtin_amdgcn_raw_buffer_load_b64(r, vo, so, 16); }
DI unsigned ld4_sc1(rsrc_t r, unsigned vo, unsigned so = 0) { return __builtin_amdgcn_raw_buffer_load_b32(r, vo, so, 16); }
DI void st16_sc1(rsrc_t r, unsigned vo, unsigned so, u32x4 v) { __builtin_amdgcn_raw_buffer_store_b128(v, r, vo + so, 0, 16); }
DI void st8_sc1(rsrc_t r, unsigned vo, unsigned so, u32x2 v) { __builtin_amdgcn_raw_buffer_store_b64(v, r, vo, so, 16); }
DI void st4_sc1(rsrc_t r, unsigned vo, unsigned so, unsigned v) { __builtin_amdgcn_raw_buffer_store_b32(v, r, vo, so, 16); }
DI void wait_words(rsrc_t r, unsigned off, unsigned tag) {
#pragma unroll 1
  for (int sp = 0; sp < SPIN_CAP; ++sp) {
    asm volatile("" ::: "memory");
    unsigned f = ld4_sc1(r, off);
    if (wave_all(f == tag)) break;
    __builtin_amdgcn_s_sleep(2);
  }
  asm volatile("" ::: "memory");
}

#define STAMP(k)
#define STAMP_DECL
#define STAMP_FLUSH()

DI void softmax_exp(const f32x16& P, float (&a)[16], float (&b)[16]) {
  const float L = 1.44269504088896341f;
#define MX3(a, b, c) __builtin_fmaxf(__builtin_fmaxf(a, b), c)
#define MN3(a, b, c) __builtin_fminf(__builtin_fminf(a, b), c)
  float mx0 = MX3(MX3(MX3(MX3(P[0], P[1], P[2]), P[3], P[4]), P[5], P[6]), P[7], P[0]);
  float mx1 = MX3(MX3(MX3(MX3(P[8], P[9], P[10]), P[11], P[12]), P[13], P[14]), P[15], P[8]);
  float mn0 = MN3(MN3(MN3(MN3(P[0], P[1], P[2]), P[3], P[4]), P[5], P[6]), P[7], P[0]);
  float mn1 = MN3(MN3(MN3(MN3(P[8], P[9], P[10]), P[11], P[12]), P[13], P[14]), P[15], P[8]);
  swap32(mx0, mx1); swap32(mn0, mn1);
  mx0 = __builtin_fmaxf(mx0 * L, mx1 * L); mn0 = __builtin_fminf(mn0 * L, mn1 * L);
  mx1 = mx0; mn1 = mn0;
  swap32(mx0, mx1); swap32(mn0, mn1);
#pragma unroll
  for (int j = 0; j < 16; ++j) {
    a[j] = __builtin_amdgcn_exp2f(__builtin_fmaf(P[j], L, j < 8 ? -mx0 : -mx1));
    b[j] = __builtin_amdgcn_exp2f(__builtin_fmaf(P[j], -L, j < 8 ? mn0 : mn1));
  }
}
DI void softmax_rcp(const float (&a)[16], const float (&b)[16], float (&r)[4]) {
  float sa0 = a[0], sa1 = a[8], sb0 = b[0], sb1 = b[8];
#pragma unroll
  for (int j = 1; j < 8; ++j) { sa0 += a[j]; sa1 += a[8 + j]; sb0 += b[j]; sb1 += b[8 + j]; }
  swap32(sa0, sa1); swap32(sb0, sb1);
  sa0 = __builtin_amdgcn_rcpf(sa0 + sa1); sb0 = __builtin_amdgcn_rcpf(sb0 + sb1);
  sa1 = sa0; sb1 = sb0;
  swap32(sa0, sa1); swap32(sb0, sb1);
  r[0] = sa0; r[1] = sb0; r[2] = sa1; r[3] = sb1;
}
DI bf16x8 scale_pack(const float* v, float r) {
  return as_frag(u32x4{pk2(v[0] * r, v[1] * r), pk2(v[2] * r, v[3] * r), pk2(v[4] * r, v[5] * r), pk2(v[6] * r, v[7] * r)});
}

DI void wo_dma(const Params& p, int q, int part, int lane) {
  const int rr = lane >> 3;
  const unsigned vo = rr * 1024 + (((lane & 7) ^ (((rr >> 1) & 3) << 1)) << 4);
  const char* src = (const char*)p.wo + 128 * q + (unsigned)(part * 32) * 8192u;
  unsigned dst = A_WO + part * 32768;
#pragma unroll 1
  for (int i = 0; i < 8; ++i) {
    dma16i<0>(src, vo, dst); dma16i<1024>(src + 8192, vo, dst); dma16i<2048>(src + 16384, vo, dst); dma16i<3072>(src + 24576, vo, dst);
    src += 32768; dst += 4096;
  }
}

DI void att_block(const Params& p, int b) {
  STAMP_DECL;
  STAMP(0);
  const int tid = threadIdx.x, lane = tid & 63, w = __builtin_amdgcn_readfirstlane(tid >> 6);
  const int xcd = b & 7, bi = b >> 3, ti = 4 * xcd + (bi & 3), q = bi >> 2;
  const int th = w & 1, hp = w >> 1;
  const int rr = lane >> 3, sl = lane & 7;
  const unsigned vx0 = rr * 1024 + ((sl ^ (rr >> 1)) << 4), vx1 = (vx0 ^ 64u) + 8192;
  const unsigned vw0 = rr * 3072 + ((sl ^ (((rr >> 1) & 1) << 2)) << 4), vw1 = vw0 + 24576;
  const char* xs = (const char*)p.x + (unsigned)(64 * ti + 16 * w) * 1024u;
  const char* wsrc = (const char*)p.wqkv + (unsigned)(16 * w) * 3072u + 128 * q;
  const unsigned ldw = w * 2048;
#define XSRC(c) (xs + (((c) + q) & 7) * 128)
#define WSRC(c) (wsrc + (unsigned)(((c) + q) & 7) * 196608u)
#define DX(c, u) dma16i<((c) & 3) * SLOT + (u) * 1024>(XSRC(c), (u) ? vx1 : vx0, ldw)
#define DK(c, u) dma16i<((c) & 3) * SLOT + 8192 + (u) * 1024>(WSRC(c) + 1024, (u) ? vw1 : vw0, ldw)
#define DW(o, c, u) dma16i<(c) * 8192 + (u) * 1024>(WSRC(c) + (o), (u) ? vw1 : vw0, ldw)
  DX(0, 0); DX(0, 1); DK(0, 0); DK(0, 1);
  if (w < 2) dma16((const char*)p.wphi + q * 2048 + w * 1024, lane * 16, A_WPHI + w * 1024);
  else dma16((const char*)p.gw, lane * 16, A_GW);
  DX(1, 0); DX(1, 1); DK(1, 0); DK(1, 1);
  DX(2, 0); DX(2, 1); DK(2, 0); DK(2, 1);
  DX(3, 0); DX(3, 1); DK(3, 0); DK(3, 1);
  SB();
  __builtin_amdgcn_s_setprio(2);
  const int r = lane & 31, h = lane >> 5, blk = (lane >> 4) & 1, i16 = lane & 15, g4 = lane >> 4, qq = i16 >> 2, pp = lane & 3;
  unsigned tag = mk_tag(load_tag(p.ws + WS_E));
  asm volatile("" : "+s"(tag));
  const rsrc_t ws = __builtin_amdgcn_make_buffer_rsrc(p.ws, 0, WS_END, 0x00020000);
  unsigned xch = A_XCH + hp * 4096 + lane * 16, xz = A_XZ + hp * 256 + g4 * 64 + (i16 & 3) * 16;
  unsigned l1k = WS_L1K + ((q * 32 + 16 * hp + g4) * 32 + ti) * 256 + i16 * 16;
  unsigned l1z = WS_L1Z + ((q * 32 + 16 * hp + 4 * i16 + g4) * 32 + ti) * 16;
  unsigned gpa = A_GP + (32 * th + r) * 16 + (2 * hp + h) * 4;
  PIN(xch); PIN(xz); PIN(l1k); PIN(l1z); PIN(gpa);
  u32x4 SEL0, SEL1;
#pragma unroll
  for (int d = 0; d < 4; ++d) {
    const int f0 = ((2 * d) & 3) + 8 * (d >> 1) + 4 * h;
    SEL0[d] = (r == f0 ? 0x3F80u : 0u) | (r == f0 + 1 ? 0x3F800000u : 0u);
    SEL1[d] = (r == f0 + 16 ? 0x3F80u : 0u) | (r == f0 + 17 ? 0x3F800000u : 0u);
  }
  PIN(SEL0); PIN(SEL1);
  unsigned xa[4];
#pragma unroll
  for (int s = 0; s < 4; ++s) xa[s] = (32 * th + r) * 128 + (((2 * (s ^ (2 * hp)) + h) ^ ((r >> 1) & 7)) << 4);
  const unsigned wa_ = (8 * h + qq) * 128 + (((4 * hp + 2 * blk + (pp >> 1)) ^ ((qq >> 1) << 2)) << 4) + 8 * (pp & 1);
  unsigned waA = wa_ + 4096 * hp, waB = wa_ + 4096 - 4096 * hp;
  unsigned gaddr = A_GW + 64 * hp + 16 * h;
  PIN(waA); PIN(waB); PIN(gaddr);
#define WFR(base, s) lds_tr2((base) + ((s) < 2 ? waA : waB) + ((s) & 1) * 2048, (base) + ((s) < 2 ? waA : waB) + ((s) & 1) * 2048 + 512)

  bf16x8 X[32], wk[4];
  u32x4 gv[2];
  f32x16 ak;
  float ga = 0.f, gb = 0.f;
#pragma unroll
  for (int i = 0; i < 16; ++i) ak[i] = 0.f;
#define RDK(c, s)                                                                                     \
  do {                                                                                                \
    X[(4 * (c) + (s)) & 31] = lds_frag(((c) & 3) * SLOT + xa[s]);                                     \
    wk[s] = WFR(((c) & 3) * SLOT + 8192, s);                                                          \
  } while (0)
#define RDG(c, s) gv[s] = *(LAS u32x4*)(gaddr + ((((c) + q) & 7) * 128 + (s) * 32))
#define GATE(c, s)                                                                                    \
  do {                                                                                                \
    const u32x4 xv_ = __builtin_bit_cast(u32x4, X[4 * (c) + (s)]);                                    \
    const unsigned x0 = xv_[0], x1 = xv_[1], x2 = xv_[2], x3 = xv_[3], g0 = gv[s][0], g1 = gv[s][1], g2 = gv[s][2], g3 = gv[s][3]; \
    ga = __builtin_amdgcn_fdot2_f32_bf16(__builtin_bit_cast(bf16x2, x0), __builtin_bit_cast(bf16x2, g0), ga, false); \
    gb = __builtin_amdgcn_fdot2_f32_bf16(__builtin_bit_cast(bf16x2, x1), __builtin_bit_cast(bf16x2, g1), gb, false); \
    ga = __builtin_amdgcn_fdot2_f32_bf16(__builtin_bit_cast(bf16x2, x2), __builtin_bit_cast(bf16x2, g2), ga, false); \
    gb = __builtin_amdgcn_fdot2_f32_bf16(__builtin_bit_cast(bf16x2, x3), __builtin_bit_cast(bf16x2, g3), gb, false); \
    PIN(ga); PIN(gb);                                                                                 \
  } while (0)
  SB();
  wait_vm_bar<13>();
  STAMP(1);
  RDK(0, 0); RDK(0, 1); RDK(0, 2); RDK(0, 3);
#define MK(c, s)                                                                                      \
  do {                                                                                                \
    SB(); ak = MFMA32(wk[s], X[4 * (c) + (s)], ak); PIN(ak); SB();                                    \
    if ((c) < 7) RDK((c) + 1, s);                                                                     \
    SB();                                                                                             \
  } while (0)
#define ITER(c, D0, D1, D2, D3)                                                                       \
  do {                                                                                                \
    wait_vm_bar<8>();                                                                                 \
    D0; RDG(c, 0); RDG(c, 1); MK(c, 0); D1; MK(c, 1); D2; MK(c, 2); D3; MK(c, 3);                     \
    GATE(c, 0); GATE(c, 1); SB();                                                                     \
  } while (0)
  ITER(0, DX(4, 0), DX(4, 1), DK(4, 0), DK(4, 1));
  ITER(1, DX(5, 0), DX(5, 1), DK(5, 0), DK(5, 1));
  ITER(2, DX(6, 0), DX(6, 1), DK(6, 0), DK(6, 1));
  ITER(3, DX(7, 0), DX(7, 1), DK(7, 0), DK(7, 1));
  ITER(4, DW(2048, 0, 0), DW(2048, 0, 1), DW(2048, 1, 0), DW(2048, 1, 1));
  ITER(5, DW(2048, 2, 0), DW(2048, 2, 1), DW(2048, 3, 0), DW(2048, 3, 1));
  ITER(6, DW(2048, 4, 0), DW(2048, 4, 1), DW(2048, 5, 0), DW(2048, 5, 1));
  bf16x8 A1, A2;
  {
    const unsigned a_ = A_WPHI + (2 * hp + blk) * 512 + (4 * h + qq) * 32 + 8 * pp;
    u32x4 wf_;
    const u32x4 z_ = {0u, 0u, 0u, 0u};
    ITER(7, (DW(2048, 6, 0), wf_ = __builtin_bit_cast(u32x4, lds_tr2(a_, a_ + 256))), DW(2048, 6, 1),
         (DW(2048, 7, 0), A1 = as_frag(blk ? z_ : wf_), A2 = as_frag(blk ? wf_ : z_)), DW(2048, 7, 1));
    PIN(A1); PIN(A2);
  }
  STAMP(2);
  f32x16 zero16;
#pragma unroll
  for (int i = 0; i < 16; ++i) zero16[i] = 0.f;
  const f32x4 zero4 = {0.f, 0.f, 0.f, 0.f};
  {
    f32x16 P = MFMA32(A1, pack8(ak, 0), zero16);
    P = MFMA32(A2, pack8(ak, 1), P);
    float ea[16], eb[16], rc[4];
    softmax_exp(P, ea, eb);
#pragma unroll
    for (int j = 0; j < 16; ++j) { PIN(ea[j]); PIN(eb[j]); }
    SB();
    wait_vm_bar<8>();
    STAMP(12);
    f32x16 av = zero16;
    bf16x8 wv[4];
#define VRD(k) wv[(k) & 3] = WFR(((k) >> 2) * 8192, (k) & 3)
#define VMM(k) av = MFMA32(X[k], wv[(k) & 3], av)
    VRD(0); VRD(1); VRD(2);
    SB();
    bf16x8 f[2][2];
    softmax_rcp(ea, eb, rc);
    f[0][0] = scale_pack(ea, rc[0]); f[0][1] = scale_pack(eb, rc[1]);
    f[1][0] = scale_pack(ea + 8, rc[2]); f[1][1] = scale_pack(eb + 8, rc[3]);
#pragma unroll
    for (int k = 0; k < 16; ++k) { if (k + 3 < 16) VRD(k + 3); VMM(k); }
#pragma unroll
    for (int k = 0; k < 16; ++k) {
      __builtin_amdgcn_sched_group_barrier(0x100, 2, 0);
      __builtin_amdgcn_sched_group_barrier(0x008, 1, 0);
      __builtin_amdgcn_sched_group_barrier(0x002, 5, 0);
    }
    PIN(av); PIN(f[0][0]); PIN(f[0][1]); PIN(f[1][0]); PIN(f[1][1]);
    SB();
    wait_vm_bar<0>();
    VRD(16); VRD(17); VRD(18);
    u32x4 t0[2], t1[2], EZ[4];
    f32x16 T;
#define VG(k) SB(); VMM(k); PIN(av); SB(); if ((k) + 3 < 32) VRD((k) + 3)
#define TG(x) SB(); T = x; PIN(T); SB()
#define SW2(e, d) { const unsigned x_ = t0[e][d], y_ = t1[e][d], z_ = t0[e][(d) + 1], u_ = t1[e][(d) + 1];                       \
      auto r_ = __builtin_amdgcn_permlane16_swap(x_, y_, false, false); auto q_ = __builtin_amdgcn_permlane16_swap(z_, u_, false, false); \
      t0[e][d] = r_[0]; t1[e][d] = r_[1]; t0[e][(d) + 1] = q_[0]; t1[e][(d) + 1] = q_[1]; PIN(t0[e]); PIN(t1[e]); }
#define EZB(d) { const unsigned e_ = (i16 == (d)) ? 0x3F803F80u : 0u; EZ[d] = u32x4{e_, e_, e_, e_}; PIN(EZ[d]); }
    TG(MFMA32(f[0][0], as_frag(SEL0), zero16)); DW(0, 0, 0);
    TG(MFMA32(f[0][1], as_frag(SEL1), T)); DW(0, 0, 1);
    VG(16); DW(0, 1, 0);
    VG(17); t0[0] = __builtin_bit_cast(u32x4, pack8(T, 0)); PIN(t0[0]);
    VG(18); t1[0] = __builtin_bit_cast(u32x4, pack8(T, 1)); PIN(t1[0]);
    TG(MFMA32(f[1][0], as_frag(SEL0), zero16)); SW2(0, 0);
    TG(MFMA32(f[1][1], as_frag(SEL1), T)); SW2(0, 2);
    VG(19); DW(0, 1, 1);
    VG(20); DW(0, 2, 0);
    VG(21); t0[1] = __builtin_bit_cast(u32x4, pack8(T, 0)); PIN(t0[1]);
    VG(22); t1[1] = __builtin_bit_cast(u32x4, pack8(T, 1)); PIN(t1[1]);
    VG(23); SW2(1, 0);
    VG(24); SW2(1, 2);
    VG(25); DW(0, 2, 1);
    VG(26); EZB(0);
    VG(27); EZB(1);
    VG(28); DW(0, 3, 0); EZB(2);
    VG(29); DW(0, 3, 1); EZB(3);
    VG(30);
    VG(31);
    SB();
    f32x4 Z = MFMA16(as_frag(t0[0]), as_frag(EZ[0]), zero4);
    Z = MFMA16(as_frag(t1[0]), as_frag(EZ[1]), Z);
    Z = MFMA16(as_frag(t0[1]), as_frag(EZ[2]), Z);
    Z = MFMA16(as_frag(t1[1]), as_frag(EZ[3]), Z);
    PIN(Z);
    u32x4 vv[2] = {__builtin_bit_cast(u32x4, pack8(av, 0)), __builtin_bit_cast(u32x4, pack8(av, 1))};
    swap16x4(vv[0], vv[1]);
    f32x4 R[4];
    R[0] = MFMA16(as_frag(t0[0]), as_frag(vv[0]), zero4); R[1] = MFMA16(as_frag(t1[0]), as_frag(vv[0]), zero4);
    R[2] = MFMA16(as_frag(t0[1]), as_frag(vv[1]), zero4); R[3] = MFMA16(as_frag(t1[1]), as_frag(vv[1]), zero4);
    *(LAS float*)gpa = ga + gb;
    if (th) {
      if (i16 < 4) *(LAS f32x4*)xz = Z;
#pragma unroll
      for (int k = 0; k < 4; ++k) *(LAS f32x4*)(xch + k * 1024) = R[k];
      bar_lgkm();
    } else {
      bar_lgkm();
      f32x4 o[4];
#pragma unroll
      for (int k = 0; k < 4; ++k) o[k] = *(LAS f32x4*)(xch + k * 1024);
      const f32x4 oz = *(LAS f32x4*)xz;
#pragma unroll
      for (int k = 0; k < 4; ++k) {
        const f32x4 t = R[k] + o[k];
        st16_sc1(ws, l1k, k * 4 * 32 * 256, u32x4{pk2(t[0], t[1]), tag, pk2(t[2], t[3]), tag});
      }
      Z += oz;
      if (i16 < 4) st16_sc1(ws, l1z, 0, u32x4{pk2(Z[0], Z[1]), tag, pk2(Z[2], Z[3]), tag});
    }
  }
  SB();
  STAMP(3);
  bf16x8 pq[2][2];
  f32x16 PQ;
  {
    f32x16 aq = zero16;
    bf16x8 wq[8];
#define QRD(k) wq[(k) & 7] = WFR(((k) >> 2) * 8192, (k) & 3)
#define QG(k, k1) SB(); aq = MFMA32(wq[(k) & 7], X[k], aq); PIN(aq); SB(); if ((k) + 4 < (k1)) QRD((k) + 4)
#define DQ(c, u, j) dma16i<(c) * 8192 + (u) * 1024 + (j) * 2048 - 2048>(WSRC(c) + (j) * 49152 - 49152, (u) ? vw1 : vw0, ldw)
    if (th) {
      wait_vm_bar<0>();
      QRD(0); QRD(1); QRD(2); QRD(3);
      DQ(4, 0, 0); QG(0, 16); DQ(4, 1, 0); QG(1, 16); DQ(4, 0, 1); QG(2, 16); DQ(4, 1, 1); QG(3, 16);
      DQ(5, 0, 0); QG(4, 16); DQ(5, 1, 0); QG(5, 16); DQ(5, 0, 1); QG(6, 16); DQ(5, 1, 1); QG(7, 16);
      DQ(6, 0, 0); QG(8, 16); DQ(6, 1, 0); QG(9, 16); DQ(6, 0, 1); QG(10, 16); DQ(6, 1, 1); QG(11, 16);
      DQ(7, 0, 0); QG(12, 16); DQ(7, 1, 0); QG(13, 16); DQ(7, 0, 1); QG(14, 16); DQ(7, 1, 1); QG(15, 16);
      SB();
      STAMP(4);
      {
      const unsigned s01 = i16 == 0 ? 0x00003F80u : i16 == 1 ? 0x3F800000u : 0u, s23 = i16 == 2 ? 0x00003F80u : i16 == 3 ? 0x3F800000u : 0u;
      const bf16x8 SUM = as_frag(u32x4{s01, 0u, s23, 0u});
      const unsigned st = hp ? 16u : 256u;
      const unsigned o = g4 * 8 * st + (hp ? 0 : i16 * 16), so = hp ? WS_L1Z + (q * 32 + ti) * 512 : WS_L1K + (q * 32 + ti) * 8192;
      u32x4 g[8];
#pragma unroll 1
      for (int sp = 0; sp < SPIN_CAP; ++sp) {
        asm volatile("" ::: "memory");
        unsigned bad = 0;
#pragma unroll
        for (int i = 0; i < 8; ++i) g[i] = ld16_sc1(ws, o, so + i * st);
#pragma unroll
        for (int i = 0; i < 8; ++i) bad |= (g[i][1] ^ tag) | (g[i][3] ^ tag);
        if (wave_all(bad == 0)) break;
        __builtin_amdgcn_s_sleep(1);
      }
      f32x4 t = zero4;
#pragma unroll
      for (int i = 0; i < 8; ++i) t = MFMA16(SUM, as_frag(g[i]), t);
      if (lane < (hp ? 1 : 16)) st16_sc1(ws, i16 * 16, WS_L2 + (q * 32 + ti) * 272 + hp * 256, u32x4{pk2(t[0], t[1]), tag, pk2(t[2], t[3]), tag});
      }
      SB();
      STAMP(5);
      wait_vm_bar<1>();
    } else {
      wait_vm_bar<5>();
      QRD(0); QRD(1); QRD(2); QRD(3);
#pragma unroll
      for (int k = 0; k < 16; ++k) { QG(k, 16); }
      SB();
      bar_lgkm();
    }
    QRD(16); QRD(17); QRD(18); QRD(19);
#pragma unroll
    for (int k = 16; k < 32; ++k) { QG(k, 32); }
    SB();
    bar_lgkm();

    PQ = MFMA32(A1, pack8(aq, 0), zero16);
    PQ = MFMA32(A2, pack8(aq, 1), PQ);
    PIN(PQ);
  }
  SB();
  if (!th) wo_dma(p, q, hp, lane);
  SB();
  {
    float ea[16], eb[16], rc[4];
    softmax_exp(PQ, ea, eb);
    softmax_rcp(ea, eb, rc);
    pq[0][0] = scale_pack(ea, rc[0]); pq[0][1] = scale_pack(eb, rc[1]);
    pq[1][0] = scale_pack(ea + 8, rc[2]); pq[1][1] = scale_pack(eb + 8, rc[3]);
    PIN(pq[0][0]); PIN(pq[0][1]); PIN(pq[1][0]); PIN(pq[1][1]);
  }
  SB();
  STAMP(6);
  {
    unsigned o = h * 272 + (r < 16 ? r : 16) * 16, oo = blk * 16384 + (i16 + 16 * h) * 16;
    PIN(o); PIN(oo);
    const unsigned so = WS_L2 + (q * 32 + hp * 16) * 272, soo = WS_O + ((4 * ti + 2 * th) * 16 + 2 * q + hp) * 1024;
    u32x4 g[8];
#pragma unroll 1
    for (int sp = 0; sp < SPIN_CAP; ++sp) {
      asm volatile("" ::: "memory");
      unsigned bad = 0;
#pragma unroll
      for (int i = 0; i < 8; ++i) g[i] = ld16_sc1(ws, o + i * 544, so);
#pragma unroll
      for (int i = 0; i < 8; ++i) bad |= (g[i][1] ^ tag) | (g[i][3] ^ tag);
      if (wave_all(bad == 0)) break;
      __builtin_amdgcn_s_sleep(1);
    }
    STAMP(8);
    f32x16 Dn[2];
#pragma unroll
    for (int e = 0; e < 2; ++e) {
      const u32x4 f0 = {g[4 * e][0], g[4 * e][2], g[4 * e + 1][0], g[4 * e + 1][2]};
      const u32x4 f1 = {g[4 * e + 2][0], g[4 * e + 2][2], g[4 * e + 3][0], g[4 * e + 3][2]};
      Dn[e] = MFMA32(as_frag(f0), pq[e][0], zero16);
      Dn[e] = MFMA32(as_frag(f1), pq[e][1], Dn[e]);
    }
#pragma unroll
    for (int e = 0; e < 2; ++e) {
      const float rd = __builtin_amdgcn_rcpf(Dn[e][8] + 1e-6f);
      u32x4 ov;
#pragma unroll
      for (int j = 0; j < 4; ++j) ov[j] = pk2(mul1(Dn[e][2 * j], rd), mul1(Dn[e][2 * j + 1], rd));
      st16_sc1(ws, oo + e * 512, soo, ov);
    }
  }
  wait_vm_bar<0>();
  if (tid == 0) st4_sc1(ws, 0, WS_FO + (ti * 8 + q) * FS, tag);
  STAMP(9);
  {
    const int key = (2 * (g4 & 1) + (qq >> 1)) & 3;
    unsigned abase[4];
#pragma unroll
    for (int ct = 0; ct < 4; ++ct)
      abase[ct] = A_WO + (16 * (g4 >> 1) + 4 * (g4 & 1) + qq) * 128 + (((2 * ct + (pp >> 1)) ^ (key << 1)) << 4) + 8 * (pp & 1);
    bf16x8 A[8][4];
#pragma unroll
    for (int k = 0; k < 8; ++k)
#pragma unroll
      for (int ct = 0; ct < 4; ++ct) { A[k][ct] = lds_tr2(abase[ct] + k * 4096, abase[ct] + k * 4096 + 1024); }
    const f32x4 gp = *(LAS f32x4*)(A_GP + (16 * w + i16) * 16);
    float gate = __builtin_amdgcn_rcpf(1.f + __builtin_amdgcn_exp2f(-1.44269504088896341f * ((gp[0] + gp[1]) + (gp[2] + gp[3]))));
    PIN(gate);
#pragma unroll
    for (int k = 0; k < 8; ++k)
#pragma unroll
      for (int ct = 0; ct < 4; ++ct) PIN(A[k][ct]);
    const char* ysrc = p.ws + WS_Y + (unsigned)(ti * 512 + 64 * q + 16 * w) * 128u;
    const unsigned yvo = rr * 128 + ((sl ^ (((rr >> 1) & 3) << 1)) << 4);
    const unsigned obase = WS_O + (unsigned)(4 * ti + w) * 16384u;
    SB();
    const unsigned fo = lane < 32 ? WS_FY + (q * 32 + lane) * FS : WS_FO + (ti * 8 + (lane & 7)) * FS;
    unsigned long long miss;
#pragma unroll 1
    for (int sp = 0; sp < SPIN_CAP; ++sp) {
      asm volatile("" ::: "memory");
      miss = __builtin_amdgcn_ballot_w64(ld4_sc1(ws, fo) != tag);
      if ((miss >> 32) == 0) break;
      __builtin_amdgcn_s_sleep(1);
    }
    asm volatile("" ::: "memory");
    STAMP(10);
    if (miss == 0) { dma16_sc1(ysrc, yvo, A_Y + w * 2048); dma16_sc1(ysrc + 1024, yvo, A_Y + w * 2048 + 1024); }
    u32x4 of[16];
#pragma unroll
    for (int k = 0; k < 16; ++k) of[k] = ld16_sc1(ws, lane * 16 + (k & 3) * 1024, obase + (k >> 2) * 4096);
    SB();
    f32x4 acc[4] = {zero4, zero4, zero4, zero4};
#pragma unroll
    for (int k = 0; k < 16; ++k) {
#pragma unroll
      for (int ct = 0; ct < 4; ++ct) {
        acc[ct] = MFMA16(A[k & 7][ct], as_frag(of[k]), acc[ct]);
        SB();
        if (k < 8) A[k][ct] = lds_tr2(abase[ct] + (k + 8) * 4096, abase[ct] + (k + 8) * 4096 + 1024);
        SB();
      }
    }
    if (miss != 0) {
      wait_words(ws, fo, tag);
      dma16_sc1(ysrc, yvo, A_Y + w * 2048); dma16_sc1(ysrc + 1024, yvo, A_Y + w * 2048 + 1024);
    }
    STAMP(7);
    wait_vm_bar<0>();
    const int ykey = (2 * g4 + (qq >> 1)) & 3;
    const unsigned ybase = A_Y + (4 * g4 + qq) * 128 + (((2 * w + (pp >> 1)) ^ (ykey << 1)) << 4) + 8 * (pp & 1);
    unsigned o2[4][2];
#pragma unroll
    for (int ct = 0; ct < 4; ++ct) {
      const bf16x4 yb = lds_tr(ybase + ct * 2048);
      float t[4];
#pragma unroll
      for (int j = 0; j < 4; ++j) { const float bv = (float)yb[j]; t[j] = bv + gate * (acc[ct][j] - bv); }
      o2[ct][0] = pk2(t[0], t[1]); o2[ct][1] = pk2(t[2], t[3]);
    }
    char* orow = (char*)p.out + (unsigned)(64 * ti + 16 * w + i16) * 1024u + 128 * q + 32 * (g4 & 1) + 16 * (g4 >> 1);
#pragma unroll
    for (int c2 = 0; c2 < 2; ++c2) {
      auto s0 = __builtin_amdgcn_permlane16_swap(o2[2 * c2][0], o2[2 * c2 + 1][0], false, false);
      auto s1 = __builtin_amdgcn_permlane16_swap(o2[2 * c2][1], o2[2 * c2 + 1][1], false, false);
      u32x4 v; v[0] = s0[0]; v[1] = s1[0]; v[2] = s0[1]; v[3] = s1[1];
      *(GAS u32x4*)(orow + 64 * c2) = v;
    }
  }
  STAMP(11);
  STAMP_FLUSH();
}

DI f32x2 cexp_l(float lr2, float li2, float l) {
  const float m = __builtin_amdgcn_exp2f(lr2 * l), ph = __builtin_amdgcn_fractf(li2 * l);
  return f32x2{m * __builtin_amdgcn_cosf(ph), m * __builtin_amdgcn_sinf(ph)};
}
#define BC "row_newbcast:%c5 row_mask:0xf bank_mask:0xf bound_ctrl:1"
template <int J> DI float bcmul(float x, float y) {
  float r;
  asm("v_mul_f32_dpp %0, %1, %2 row_newbcast:%3 row_mask:0xf bank_mask:0xf bound_ctrl:1" : "=v"(r) : "v"(x), "v"(y), "n"(J));
  return r;
}
template <int J> DI void bccmul(float ar, float ai, float br, float bi, float& re, float& im) {
  asm("v_mul_f32_dpp %0, %4, %2 row_newbcast:%6 row_mask:0xf bank_mask:0xf bound_ctrl:1\n\t"
      "v_mul_f32_dpp %1, %5, %2 row_newbcast:%6 row_mask:0xf bank_mask:0xf bound_ctrl:1\n\t"
      "v_fmac_f32_dpp %0, -%5, %3 row_newbcast:%6 row_mask:0xf bank_mask:0xf bound_ctrl:1\n\t"
      "v_fmac_f32_dpp %1, %4, %3 row_newbcast:%6 row_mask:0xf bank_mask:0xf bound_ctrl:1"
      : "=&v"(re), "=&v"(im) : "v"(ar), "v"(ai), "v"(br), "v"(bi), "n"(J));
}
struct Mode { float lr2, li2, wr, wi, gr, gi, fr, fi; };
template <int J> DI void kslot(const Mode& m, float rho, float r8, float r8n, unsigned& a0, unsigned& a1, unsigned& b0, unsigned& b1) {
  const float pa = __builtin_amdgcn_fractf(bcmul<J>(m.li2, rho)), pb = bcmul<J>(m.li2, r8n);
  const float ma = __builtin_amdgcn_exp2f(bcmul<J>(m.lr2, rho)), mb = __builtin_amdgcn_exp2f(bcmul<J>(m.lr2, r8));
  const float er = ma * __builtin_amdgcn_cosf(pa), ei = ma * __builtin_amdgcn_sinf(pa);
  const float yr = mb * __builtin_amdgcn_cosf(pb), yi = mb * __builtin_amdgcn_sinf(pb);
  float xr, xi, ur, ui, zr, zi;
  bccmul<J>(er, ei, m.wr, m.wi, xr, xi);
  bccmul<J>(yr, yi, m.fr, m.fi, zr, zi);
  bccmul<J>(xr, xi, m.gr, m.gi, ur, ui);
  a0 = pk2(xr, xi); a1 = pk2(ur, ui); b0 = pk2(yr, yi); b1 = pk2(zr, zi);
}
DI void split_pair(unsigned v, int h, float& lo_arr, float& hi_arr) {
  auto r = __builtin_amdgcn_permlane32_swap(v, v, false, false);
  const unsigned r0 = r[0], r1 = r[1], sh = h ? 0u : 16u;
  lo_arr = u2f((r0 << sh) & 0xFFFF0000u); hi_arr = u2f((r1 << sh) & 0xFFFF0000u);
}
DI float half_bf16(unsigned v, int idx) { return u2f((idx & 1) ? (v & 0xFFFF0000u) : (v << 16)); }

DI void ssm_block(const Params& p, int sidx) {
  STAMP_DECL;
  STAMP(0);
  const int tid = threadIdx.x, lane = tid & 63, w = __builtin_amdgcn_readfirstlane(tid >> 6);
  const int xcd = sidx & 7, bi = sidx >> 3;
  const int i16 = lane & 15, g = lane >> 4, qq = i16 >> 2, pp = lane & 3;
  const int chl = w >> 1, kh = w & 1, ch = 64 * xcd + 2 * bi + chl;
  __builtin_amdgcn_s_setprio(3);
  {
    const char* xs = (const char*)p.x + (unsigned)(64 * bi + 16 * w) * 1024u + 128 * xcd;
    const unsigned vo = (lane >> 3) * 1024 + (lane & 7) * 16;
    dma16(xs, vo, S_XT + w * 2048);
    dma16(xs + 8192, vo, S_XT + w * 2048 + 1024);
  }
  const int n = tid & 127, hh = lane >> 5;
  const unsigned po = (ch * 128 + 64 * kh) * 2 + (lane & 31) * 4;
  const unsigned pA = *(const GAS unsigned*)((const char*)(hh ? p.aim : p.are) + po);
  const unsigned pB = *(const GAS unsigned*)((const char*)(hh ? p.bim : p.bre) + po);
  const unsigned pC = *(const GAS unsigned*)((const char*)(hh ? p.cim : p.cre) + po);
  SB();
  __builtin_amdgcn_s_setprio(0);
  unsigned epoch, sdt, sds;
  asm volatile("s_load_dword %0, %3, 0x0\n\ts_load_dword %1, %4, 0x0\n\ts_load_dword %2, %5, 0x0\n\ts_waitcnt lgkmcnt(0)"
               : "=&s"(sdt), "=&s"(sds), "=&s"(epoch)
               : "s"((const char*)p.logdt + (ch >> 1) * 4), "s"((const char*)p.dskip + (ch >> 1) * 4), "s"(p.ws + WS_E) : "memory");
  const float ldt = half_bf16(sdt, ch), dsk = half_bf16(sds, ch);
  unsigned tag = mk_tag(epoch);
  asm volatile("" : "+s"(tag));
  const rsrc_t ws = __builtin_amdgcn_make_buffer_rsrc(p.ws, 0, WS_END, 0x00020000);
  const int r2_ = i16 == 11 ? 8 : 480 + 4 * i16, r3_ = 1024 + 8 * (i16 & 1) + 256 * (i16 & 2);
  float rho = (float)(i16 < 8 ? i16 : i16 < 12 ? r2_ : r3_);
  float r8 = 8.f * i16, r8n = -r8;
  PIN(rho); PIN(r8); PIN(r8n);
  const float dt = (float)(__bf16)__builtin_expf(ldt);
  STAMP(1);
  Mode md;
  {
    float are, aim, bre, bim, cre, cim;
    split_pair(pA, hh, are, aim); split_pair(pB, hh, bre, bim); split_pair(pC, hh, cre, cim);
    md.lr2 = dt * are * 1.44269504088896341f;
    md.li2 = dt * aim * 0.15915494309189535f;
    md.li2 -= __builtin_rintf(md.li2);
    const float dbr = dt * bre, dbi = dt * bim;
    md.wr = cre * dbr - cim * dbi; md.wi = cre * dbi + cim * dbr;
    const f32x2 G = cexp_l(md.lr2, md.li2, 256.f), F = cexp_l(md.lr2, -md.li2, 128.f);
    md.gr = G[0]; md.gi = G[1]; md.fr = F[0]; md.fi = F[1];
    asm volatile("s_nop 1" : "+v"(md.lr2), "+v"(md.li2), "+v"(md.wr), "+v"(md.wi), "+v"(md.gr), "+v"(md.gi), "+v"(md.fr), "+v"(md.fi));
  }
  const f32x4 zero4 = {0.f, 0.f, 0.f, 0.f};
  f32x4 D[2][2] = {{zero4, zero4}, {zero4, zero4}};
#define KSTEP(s)                                                                                      \
  do {                                                                                                \
    unsigned a0[4], a1[4], b0[4], b1[4];                                                              \
    kslot<4 * (s) + 0>(md, rho, r8, r8n, a0[0], a1[0], b0[0], b1[0]);                        \
    kslot<4 * (s) + 1>(md, rho, r8, r8n, a0[1], a1[1], b0[1], b1[1]);                        \
    kslot<4 * (s) + 2>(md, rho, r8, r8n, a0[2], a1[2], b0[2], b1[2]);                        \
    kslot<4 * (s) + 3>(md, rho, r8, r8n, a0[3], a1[3], b0[3], b1[3]);                        \
    const bf16x8 A0 = as_frag(u32x4{a0[0], a0[1], a0[2], a0[3]}), A1 = as_frag(u32x4{a1[0], a1[1], a1[2], a1[3]}); \
    const bf16x8 B0 = as_frag(u32x4{b0[0], b0[1], b0[2], b0[3]}), B1 = as_frag(u32x4{b1[0], b1[1], b1[2], b1[3]}); \
    D[0][0] = MFMA16(A0, B0, D[0][0]); D[0][1] = MFMA16(A0, B1, D[0][1]);                             \
    D[1][0] = MFMA16(A1, B0, D[1][0]); D[1][1] = MFMA16(A1, B1, D[1][1]);                             \
    PIN(D[0][0]); PIN(D[0][1]); PIN(D[1][0]); PIN(D[1][1]);                                           \
  } while (0)
  KSTEP(0);
  wait_vm_bar<0>();
  {
    const unsigned a = S_XT + (8 * g + qq) * 128 + (16 * w + 4 * pp) * 2;
    const unsigned vo = i16 * 4096 + g * 16, so = WS_XT + (64 * xcd + 16 * w) * 4096 + 128 * bi;
    st16_sc1(ws, vo, so, __builtin_bit_cast(u32x4, lds_tr2(a, a + 512)));
    st16_sc1(ws, vo + 64, so, __builtin_bit_cast(u32x4, lds_tr2(a + 4096, a + 4096 + 512)));
  }
  SB();
  KSTEP(1);
  KSTEP(2);
  wait_vm_bar<0>();
  if (tid == 0) st4_sc1(ws, 0, WS_FX + (xcd * 32 + bi) * FS, tag);
  STAMP(8);
  KSTEP(3);
  STAMP(2);
  *(LAS f32x4*)(S_XCH + w * 2048 + lane * 16) = kh ? D[0][0] : D[0][1];
  *(LAS f32x4*)(S_XCH + w * 2048 + 1024 + lane * 16) = kh ? D[1][0] : D[1][1];
  wait_words(ws, WS_FX + (xcd * 32 + (lane & 31)) * FS, tag);
  asm volatile("" ::: "memory");
  const u32x4 ua_ = ld16_sc1(ws, n * 32, WS_XT + ch * 4096), ub_ = ld16_sc1(ws, n * 32 + 16, WS_XT + ch * 4096);
  const u32x2 uc_ = ld8_sc1(ws, n * 32 + 32, WS_XT + ch * 4096);
  STAMP(9);
  bar_lgkm();
  {
    const f32x4 k0 = (kh ? D[0][1] : D[0][0]) + *(LAS f32x4*)(S_XCH + (w ^ 1) * 2048 + lane * 16);
    const f32x4 k1 = (kh ? D[1][1] : D[1][0]) + *(LAS f32x4*)(S_XCH + (w ^ 1) * 2048 + 1024 + lane * 16);
    float t0 = k1[0], t1 = t0, u0 = k1[3], u1 = u0;
    { auto r_ = __builtin_amdgcn_permlane16_swap(f2u(t0), f2u(t1), false, false); const unsigned x_ = r_[1]; t1 = u2f(x_); }
    swap32(u0, u1); u0 = u1;
    { auto r_ = __builtin_amdgcn_permlane16_swap(f2u(u0), f2u(u1), false, false); const unsigned x_ = r_[0]; u0 = u2f(x_); }
    const unsigned ka = S_KR + chl * KSZ + (248 - 128 * kh - 8 * i16) * 2;
    if (g < 2) {
      const float nx = g ? u0 : t1;
      *(LAS u32x2*)(ka + 2 * 1792 + 8 - 8 * g) = u32x2{pk2(k0[3], k0[2]), pk2(k0[1], k0[0])};
      *(LAS u32x2*)(ka + 2 * 1536 + 8 - 8 * g) = u32x2{pk2(nx, k1[2]), pk2(k1[0], k1[0])};
    } else if (g == 2) {
      *(LAS u32x4*)(ka + 2 * 1280) = u32x4{pk2(k0[2], k0[2]), pk2(k0[1], k0[1]), pk2(k0[1], k0[0]), pk2(k0[0], k0[0])};
      *(LAS u32x4*)(ka + 2 * 1024) = u32x4{pk2(k1[2], k1[2]), pk2(k1[1], k1[1]), pk2(k1[1], k1[0]), pk2(k1[0], k1[0])};
    } else {
      const bool odd = i16 & 1;
      const unsigned c0 = pk2(k0[0], k0[0]), c1 = pk2(k1[0], k1[0]), c2 = pk2(k0[2], k0[2]), c3 = pk2(k1[2], k1[2]);
      *(LAS u32x4*)(ka + 2 * 768) = u32x4{pk2(k0[1], k0[1]), pk2(k0[1], odd ? k0[1] : k0[0]), c0, c0};
      *(LAS u32x4*)(ka + 2 * 512) = u32x4{pk2(k1[1], k1[1]), pk2(k1[1], odd ? k1[1] : k1[0]), c1, c1};
      *(LAS u32x4*)(ka + 2 * 256) = u32x4{pk2(k0[3], k0[3]), pk2(k0[3], odd ? k0[3] : k0[2]), c2, c2};
      *(LAS u32x4*)(ka) = u32x4{pk2(k1[3], k1[3]), pk2(k1[3], odd ? k1[3] : k1[2]), c3, c3};
    }
    if (kh == 0 && lane < 30) *(LAS u32x4*)(S_KR + chl * KSZ + 4096 + lane * 16) = u32x4{0u, 0u, 0u, 0u};
  }
  STAMP(3);
  {
    const unsigned d[10] = {ua_[0], ua_[1], ua_[2], ua_[3], ub_[0], ub_[1], ub_[2], ub_[3], uc_[0], uc_[1]};
    unsigned e[9];
#pragma unroll
    for (int k = 0; k < 9; ++k) e[k] = __builtin_amdgcn_alignbit(d[k + 1], d[k], 16);
    const unsigned u = chl * USZ + 32 + n * 32;
    *(LAS u32x4*)(u) = ua_; *(LAS u32x4*)(u + 16) = ub_;
    *(LAS u32x4*)(u + UCP) = u32x4{e[0], e[1], e[2], e[3]}; *(LAS u32x4*)(u + UCP + 16) = u32x4{e[4], e[5], e[6], e[7]};
    *(LAS u32x4*)(u + 2 * UCP) = u32x4{d[1], d[2], d[3], d[4]}; *(LAS u32x4*)(u + 2 * UCP + 16) = u32x4{d[5], d[6], d[7], d[8]};
    *(LAS u32x4*)(u + 3 * UCP) = u32x4{e[1], e[2], e[3], e[4]}; *(LAS u32x4*)(u + 3 * UCP + 16) = u32x4{e[5], e[6], e[7], e[8]};
    if (n == 0) {
      const u32x4 z = {0u, 0u, 0u, 0u};
      const unsigned u0 = chl * USZ;
      *(LAS u32x4*)(u0) = z; *(LAS u32x4*)(u0 + 16) = z;
      *(LAS u32x4*)(u0 + UCP) = z; *(LAS u32x4*)(u0 + UCP + 16) = u32x4{0u, 0u, 0u, d[0] << 16};
      *(LAS u32x4*)(u0 + 2 * UCP) = z; *(LAS u32x4*)(u0 + 2 * UCP + 16) = u32x4{0u, 0u, 0u, d[0]};
      *(LAS u32x4*)(u0 + 3 * UCP) = z; *(LAS u32x4*)(u0 + 3 * UCP + 16) = u32x4{0u, 0u, d[0] << 16, e[0]};
    }
  }
  bar_lgkm();
  STAMP(4);
  f32x4 acc[8];
#pragma unroll
  for (int c = 0; c < 8; ++c) acc[c] = f32x4{0.f, 0.f, 0.f, 0.f};
  {
    const int sg = (i16 + 1) & 3;
    const unsigned ua = chl * USZ + sg * UCP + (i16 + 1 - sg + 8 * g) * 2 + (w & 1) * 256;
    const unsigned kb = S_KR + chl * KSZ + (240 - 16 * i16 + 8 * g) * 2 + (w & 1) * 256;
    unsigned ub = ua + 8;
    PIN(ub);
    u32x2 Al[2][8], Ah[2][8];
    bf16x8 G[2][8];
#define CRD(bf, rho, i)                                                                               \
  do {                                                                                                \
    if ((i) % 3 == 0) Al[bf][(i) / 3] = *(LAS u32x2*)(ua + 64 * (rho) + 512 * ((i) / 3));             \
    else if ((i) % 3 == 1) Ah[bf][(i) / 3] = *(LAS u32x2*)(ub + 64 * (rho) + 512 * ((i) / 3));        \
    else G[bf][(i) / 3] = lds_frag(kb + 64 * (rho) + 512 * (7 - (i) / 3));                            \
  } while (0)
#pragma unroll
    for (int i = 0; i < 24; ++i) { CRD(0, 0, i); SB(); }
#pragma unroll
    for (int rho = 0; rho < 4; ++rho) {
      int cnt = 0;
#pragma unroll
      for (int dsum = 0; dsum < 8; ++dsum) {
#pragma unroll
        for (int k = 0; k <= 2 * dsum; ++k) {
          const int m = k <= dsum ? k : dsum, nn = k <= dsum ? dsum : 2 * dsum - k;
          if (m + nn < 8) {
            SB();
            const u32x4 af = {Al[rho & 1][m][0], Al[rho & 1][m][1], Ah[rho & 1][m][0], Ah[rho & 1][m][1]};
            acc[m + nn] = MFMA16(as_frag(af), G[rho & 1][nn], acc[m + nn]);
            PIN(acc[m + nn]);
            SB();
            if (rho < 3 && cnt % 3 != 2 && cnt - cnt / 3 < 24) CRD((rho + 1) & 1, rho + 1, cnt - cnt / 3);
            ++cnt;
          }
        }
      }
    }
    SB();
  }
  STAMP(5);
  {
    f32x4 keep[4];
    const unsigned ro = S_RED + w * 4096 + lane * 16, ri = S_RED + (w ^ 1) * 4096 + lane * 16;
    if (w & 1) {
#pragma unroll
      for (int k = 0; k < 4; ++k) { *(LAS f32x4*)(ro + k * 1024) = acc[k]; keep[k] = acc[4 + k]; }
    } else {
#pragma unroll
      for (int k = 0; k < 4; ++k) { *(LAS f32x4*)(ro + k * 1024) = acc[4 + k]; keep[k] = acc[k]; }
    }
    bar_lgkm();
    const int c0 = 4 * (w & 1);
    unsigned o2[4][2];
#pragma unroll
    for (int k = 0; k < 4; ++k) {
      const f32x4 t = *(LAS f32x4*)(ri + k * 1024);
      const u32x2 uu = *(LAS u32x2*)(chl * USZ + (256 * (c0 + k) + 16 * i16 + 4 * g + 16) * 2);
      const unsigned u0 = uu[0], u1 = uu[1];
      o2[k][0] = pk2(keep[k][0] + t[0] + dsk * u2f(u0 << 16), keep[k][1] + t[1] + dsk * u2f(u0 & 0xFFFF0000u));
      o2[k][1] = pk2(keep[k][2] + t[2] + dsk * u2f(u1 << 16), keep[k][3] + t[3] + dsk * u2f(u1 & 0xFFFF0000u));
    }
#pragma unroll
    for (int k = 0; k < 2; ++k) {
      auto s0 = __builtin_amdgcn_permlane16_swap(o2[2 * k][0], o2[2 * k + 1][0], false, false);
      auto s1 = __builtin_amdgcn_permlane16_swap(o2[2 * k][1], o2[2 * k + 1][1], false, false);
      u32x4 v; v[0] = s0[0]; v[1] = s1[0]; v[2] = s0[1]; v[3] = s1[1];
      const int t = 256 * (c0 + 2 * k + (g & 1)) + 16 * i16 + 8 * (g >> 1);
      st16_sc1(ws, (t >> 6) * 65536 + (t & 63) * 2, WS_Y + ch * 128, v);
    }
  }
  wait_vm_bar<0>();
  if (tid == 0) st4_sc1(ws, 0, WS_FY + (xcd * 32 + bi) * FS, tag);
  STAMP(6);
  if (sidx == 0 && w == 0) {
#pragma unroll 1
    for (int k = 0; k < 4; ++k) wait_words(ws, WS_FX + (k * 64 + lane) * FS, tag);
    wait_words(ws, WS_L2 + (lane & 7) * 32 * 272 + 4, tag);
    if (lane == 0) st4_sc1(ws, 0, WS_E, epoch + 1u);
  }
  STAMP(7);
  STAMP_FLUSH();
}

NOPK __global__ void __launch_bounds__(256, 2) hh_s4_mix_kernel(Params p) {
  asm volatile("" : "+s"(p.x), "+s"(p.wqkv), "+s"(p.wphi), "+s"(p.wo), "+s"(p.gw), "+s"(p.out), "+s"(p.ws));
  asm volatile("" : "+s"(p.are), "+s"(p.aim), "+s"(p.bre), "+s"(p.bim), "+s"(p.cre), "+s"(p.cim), "+s"(p.logdt), "+s"(p.dskip));
  if (blockIdx.x < 256) att_block(p, blockIdx.x);
  else ssm_block(p, blockIdx.x - 256);
}

extern "C" void kernel_launch(void* const* d_in, const int* in_sizes, int n_in, void* d_out, int out_size, void* d_ws,
                              size_t ws_size, hipStream_t stream) {
  Params p;
  p.x = (const u16*)d_in[0]; p.wqkv = (const u16*)d_in[1]; p.wphi = (const u16*)d_in[2]; p.wo = (const u16*)d_in[3];
  p.are = (const u16*)d_in[5]; p.aim = (const u16*)d_in[6]; p.bre = (const u16*)d_in[7]; p.bim = (const u16*)d_in[8];
  p.cre = (const u16*)d_in[9]; p.cim = (const u16*)d_in[10]; p.logdt = (const u16*)d_in[11]; p.dskip = (const u16*)d_in[12];
  p.gw = (const u16*)d_in[13];
  p.out = (u16*)d_out;
  p.ws = (char*)d_ws;
  (void)hipFuncSetAttribute((const void*)hh_s4_mix_kernel, hipFuncAttributeMaxDynamicSharedMemorySize, LDS_BYTES);
  hh_s4_mix_kernel<<<512, 256, LDS_BYTES, stream>>>(p);
}
```
